```python
import torch

def _softmax(x, axis=-1):
    return torch.softmax(x, dim=axis)
def _gelu(x):
    return torch.nn.functional.gelu(x, approximate="tanh")
def _silu(x):
    return torch.nn.functional.silu(x)
def _sigmoid(x):
    return torch.sigmoid(x)
def _layernorm(x, axis=-1, eps=1e-5):
    m = x.mean(dim=axis, keepdim=True)
    v = x.var(dim=axis, keepdim=True, unbiased=False)
    return (x - m) / torch.sqrt(v + eps)
def _scatter_add(tgt, idx, src, axis=0):
    return tgt.clone().index_add_(axis, idx, src)
def _segment_sum(src, seg, n):
    out = torch.zeros((n,) + src.shape[1:], dtype=src.dtype)
    return out.index_add_(0, seg, src)
def _pad(x, pw):
    flat = tuple(v for pair in reversed(pw) for v in pair)
    return torch.nn.functional.pad(x, flat)

kernel_name = 'parallel_sum__mha_block_cached__mixer_channel_mlp__decode'

def setup_inputs():

    _w = (lambda a: (lambda t: t.to(torch.bfloat16) if t.is_floating_point() else t)(torch.as_tensor(a)))
    def _A_setup():
        B, Sq, D = 8, 1, 1024
        H, Sk, Dh = 32, 4096, 128
        return {
            "x": _w((lambda *s: torch.randn(*s, dtype=torch.bfloat16) * 0.05)(B, Sq, D)),
            "k_cache": _w((lambda *s: torch.randn(*s, dtype=torch.bfloat16) * 3.2)(B, H, Sk, Dh)),
            "v_cache": _w((lambda *s: torch.randn(*s, dtype=torch.bfloat16) * 0.4)(B, H, Sk, Dh)),
            "wq": _w((lambda *s: torch.randn(*s, dtype=torch.bfloat16) * 3.2)(D, H * Dh) * (D ** -0.5)),
            "wo": _w((lambda *s: torch.randn(*s, dtype=torch.bfloat16) * 0.4)(H * Dh, D) * ((H * Dh) ** -0.5)),
        }
    _A_in = _A_setup()

    def _B_setup():
        B, S, D, F = 8, 1, 1024, 3584
        return {
            "x": _w((lambda *s: torch.randn(*s, dtype=torch.bfloat16) * 0.05)(B, S, D)),
            "w1": _w((lambda *s: torch.randn(*s, dtype=torch.bfloat16) * 3.2)(D, F) * (D ** -0.5)),
            "b1": _w((lambda *s: torch.randn(*s, dtype=torch.bfloat16) * 1.6)(F)),
            "w2": _w((lambda *s: torch.randn(*s, dtype=torch.bfloat16) * 0.1)(F, D) * (F ** -0.5)),
            "b2": _w((lambda *s: torch.randn(*s, dtype=torch.bfloat16) * 0.1)(D)),
        }
    _B_in = _B_setup()

    return {
        "A_x": _A_in["x"],
        "A_k_cache": _A_in["k_cache"],
        "A_v_cache": _A_in["v_cache"],
        "A_wq": _A_in["wq"],
        "A_wo": _A_in["wo"],
        "B_x": _B_in["x"],
        "B_w1": _B_in["w1"],
        "B_b1": _B_in["b1"],
        "B_w2": _B_in["w2"],
        "B_b2": _B_in["b2"],
    }

def reference(A_x, A_k_cache, A_v_cache, A_wq, A_wo, B_x, B_w1, B_b1, B_w2, B_b2):
    def _A_ref(x, k_cache, v_cache, wq, wo):
        B, Sq, D = x.shape
        H, Dh = 32, 128
        q = (lambda x, *d: x.permute(*d))((x @ wq).reshape(B, Sq, H, Dh), 0, 2, 1, 3)
        scores = torch.einsum("bhqd,bhkd->bhqk", q, k_cache) * (Dh ** -0.5)
        p = _softmax(scores, axis=-1)
        o = torch.einsum("bhqk,bhkd->bhqd", p, v_cache)
        return (lambda x, *d: x.permute(*d))(o, 0, 2, 1, 3).reshape(B, Sq, H * Dh) @ wo

    def _B_ref(x, w1, b1, w2, b2):
        y = _layernorm(x, axis=-1)
        h = _gelu(torch.einsum("bsd,df->bsf", y, w1) + b1)
        return torch.einsum("bsf,fd->bsd", h, w2) + b2

    x = A_x
    xn = _layernorm(x, axis=-1)
    return x + _A_ref(xn, A_k_cache, A_v_cache, A_wq, A_wo) + _B_ref(xn, B_w1, B_b1, B_w2, B_b2)
```

```cpp
#include <hip/hip_runtime.h>
#include <hip/hip_bf16.h>
#include <stdint.h>

#define MIMIC 1
#define USE_MEMSET 0

namespace {

constexpr int NB = 8, DM = 1024, NH = 32, SK = 4096, DH = 128, FF = 3584, HD = NH * DH;
constexpr int NSW = 8, NHW = 8, NT = (NSW + NHW) * 64;
constexpr int CHK = 32;
constexpr int NCH = SK / CHK / NSW;
constexpr unsigned SPIN_MAX = 200000u;

constexpr size_t WS_G = 0;
constexpr size_t WS_OG = 4096;
constexpr size_t OG_BYTES = (size_t)NB * (HD / 2) * 8;
constexpr size_t WS_HG = WS_OG + OG_BYTES;
constexpr size_t HG_BYTES = (size_t)NB * (FF / 2) * 8;
constexpr size_t WS_END = WS_HG + HG_BYTES;

typedef __attribute__((ext_vector_type(2))) __bf16 bf16x2;
typedef __attribute__((ext_vector_type(2))) float f32x2;
typedef __attribute__((ext_vector_type(4))) float f32x4;
typedef __attribute__((ext_vector_type(4))) unsigned u32x4;
typedef __attribute__((ext_vector_type(2))) unsigned u32x2;
typedef __attribute__((address_space(1))) unsigned gu32;
typedef __attribute__((address_space(1))) unsigned long long gu64;
#define RLX_AGENT __ATOMIC_RELAXED, __HIP_MEMORY_SCOPE_AGENT

__device__ __forceinline__ float dot2(unsigned a, unsigned b, float acc) {
  return __builtin_amdgcn_fdot2_f32_bf16(__builtin_bit_cast(bf16x2, a), __builtin_bit_cast(bf16x2, b), acc, false);
}
__device__ __forceinline__ unsigned pack_bf16(float lo, float hi) {
  f32x2 v = {lo, hi};
  return __builtin_bit_cast(unsigned, __builtin_convertvector(v, bf16x2));
}
__device__ __forceinline__ float bf_lo(unsigned u) { return __builtin_bit_cast(float, u << 16); }
__device__ __forceinline__ float bf_hi(unsigned u) { return __builtin_bit_cast(float, u & 0xffff0000u); }
__device__ __forceinline__ float bfr(float x) { return bf_lo(pack_bf16(x, 0.f)); }
template <int CTRL> __device__ __forceinline__ float dpp(float x) {
  return __builtin_bit_cast(float, __builtin_amdgcn_mov_dpp(__builtin_bit_cast(int, x), CTRL, 0xf, 0xf, true));
}
constexpr int XOR1 = 0xB1, XOR2 = 0x4E, XOR7 = 0x141, XOR8 = 0x128;
__device__ __forceinline__ float row_sum(float x) {
  x += dpp<XOR1>(x); x += dpp<XOR2>(x); x += dpp<XOR7>(x); x += dpp<XOR8>(x); return x;
}
__device__ __forceinline__ float xrow_sum(float x) {
  auto s = __builtin_amdgcn_permlane16_swap(__float_as_uint(x), __float_as_uint(x), false, false);
  x = __uint_as_float(s[0]) + __uint_as_float(s[1]);
  auto t = __builtin_amdgcn_permlane32_swap(__float_as_uint(x), __float_as_uint(x), false, false);
  return __uint_as_float(t[0]) + __uint_as_float(t[1]);
}
__device__ __forceinline__ float xrow_max(float x) {
  auto s = __builtin_amdgcn_permlane16_swap(__float_as_uint(x), __float_as_uint(x), false, false);
  x = fmaxf(__uint_as_float(s[0]), __uint_as_float(s[1]));
  auto t = __builtin_amdgcn_permlane32_swap(__float_as_uint(x), __float_as_uint(x), false, false);
  return fmaxf(__uint_as_float(t[0]), __uint_as_float(t[1]));
}
__device__ __forceinline__ float wave_sum(float x) { return xrow_sum(row_sum(x)); }
__device__ __forceinline__ unsigned perm_lo(unsigned hi_src, unsigned lo_src) { return __builtin_amdgcn_perm(hi_src, lo_src, 0x05040100u); }
__device__ __forceinline__ unsigned perm_hi(unsigned hi_src, unsigned lo_src) { return __builtin_amdgcn_perm(hi_src, lo_src, 0x07060302u); }
__device__ __forceinline__ u32x4 ld16(const void* p) { return *reinterpret_cast<const u32x4*>(p); }
__device__ __forceinline__ u32x4 ld16nt(const void* p) { return __builtin_nontemporal_load(reinterpret_cast<const u32x4*>(p)); }

__device__ __forceinline__ u32x4 ldkv(__amdgpu_buffer_rsrc_t rs, unsigned vo, unsigned so, int i) {
  return __builtin_amdgcn_raw_buffer_load_b128(rs, vo + (i & 3) * 1024, so + (i >> 2) * 4096, 2);
}

struct LnStat { float sub, den; };
__device__ __forceinline__ LnStat ln_stat(const float (&v)[16]) {
  float s = 0.f;
#pragma unroll
  for (int i = 0; i < 16; ++i) s += v[i];
  const float mean = wave_sum(s) * (1.f / DM);
  float q = 0.f;
#pragma unroll
  for (int i = 0; i < 16; ++i) { const float d = v[i] - mean; q = fmaf(d, d, q); }
  const float var = wave_sum(q) * (1.f / DM);
  LnStat st;
  st.sub = bfr(mean);
  st.den = bfr(sqrtf(bfr(bfr(var) + 1e-5f)));
  return st;
}
__device__ __forceinline__ float ln_apply(float x, LnStat st) {
  return bfr(x - st.sub) / st.den;
}
__device__ __forceinline__ void unpack16(u32x4 a, u32x4 b, float (&v)[16]) {
#pragma unroll
  for (int j = 0; j < 4; ++j) { v[2 * j] = bf_lo(a[j]); v[2 * j + 1] = bf_hi(a[j]); v[8 + 2 * j] = bf_lo(b[j]); v[8 + 2 * j + 1] = bf_hi(b[j]); }
}
template <int N, int MASK> __device__ __forceinline__ void rs_step(float* v, int lane) {
  const bool up = (lane & MASK) != 0;
#pragma unroll
  for (int i = 0; i < N / 2; ++i) {
    const float send = up ? v[i] : v[i + N / 2];
    const float keep = up ? v[i + N / 2] : v[i];
    v[i] = keep + __shfl_xor(send, MASK, 64);
  }
}

struct Params {
  const unsigned short* x; const unsigned short* kc; const unsigned short* vc; const unsigned short* wq; const unsigned short* wo;
  const unsigned short* w1; const unsigned short* b1; const unsigned short* w2; const unsigned short* b2;
  unsigned short* out; unsigned char* ws;
};

constexpr int L_QP = 0;
constexpr int L_Y = L_QP + 16 * 128;
constexpr int L_XN = L_Y + 4096;
constexpr int L_REC = L_XN + 512;
constexpr int REC = 132;
constexpr int L_HP = L_REC + NSW * REC;
constexpr int L_FP = L_HP + NHW * 128;
constexpr int L_CNT = L_FP + NHW * 32;
constexpr int L_END = L_CNT + 4;

__device__ __forceinline__ void gather_acc(__amdgpu_buffer_rsrc_t rs, unsigned off0, unsigned bstride, unsigned tag,
                                           const unsigned (&wp)[32], float (&acc)[32], bool active) {
  unsigned pend = active ? 0xffu : 0u;
  for (unsigned spins = 0; spins < SPIN_MAX; ++spins) {
    u32x4 g[8];
#pragma unroll
    for (int j = 0; j < 8; ++j) {
      const unsigned off = ((pend >> j) & 1u) ? off0 + (j >> 1) * bstride + (j & 1) * 16u : 0xfffffff0u;
      g[j] = __builtin_amdgcn_raw_buffer_load_b128(rs, off, 0, 16);
    }
    __builtin_amdgcn_sched_barrier(0);
#pragma unroll
    for (int j = 0; j < 8; ++j) {
      const bool ok = ((pend >> j) & 1u) && g[j][1] == tag && g[j][3] == tag;
      if (__any(ok)) {
        const unsigned a0 = ok ? g[j][0] : 0u, a1 = ok ? g[j][2] : 0u;
#pragma unroll
        for (int c = 0; c < 8; ++c) {
          float t = acc[(j >> 1) * 8 + c];
          t = dot2(wp[((j & 1) * 2) * 8 + c], a0, t);
          t = dot2(wp[((j & 1) * 2 + 1) * 8 + c], a1, t);
          acc[(j >> 1) * 8 + c] = t;
        }
        if (ok) pend &= ~(1u << j);
      }
    }
    if (!__any(pend != 0u)) break;
    __builtin_amdgcn_s_sleep(2);
  }
}
__device__ __forceinline__ void pair_rows(const u32x4 (&w)[8], unsigned (&wp)[32]) {
#pragma unroll
  for (int rp = 0; rp < 4; ++rp)
#pragma unroll
    for (int j = 0; j < 4; ++j) {
      wp[rp * 8 + 2 * j] = perm_lo(w[2 * rp + 1][j], w[2 * rp][j]);
      wp[rp * 8 + 2 * j + 1] = perm_hi(w[2 * rp + 1][j], w[2 * rp][j]);
    }
}

__device__ __forceinline__ void q_partial(const u32x4 (&wqr)[16], const float* lds, int k0, float (&qa)[8]) {
  const u32x4* xs = reinterpret_cast<const u32x4*>(lds + L_XN + (k0 >> 1));
  const u32x4 xa = xs[0], xb = xs[1];
#pragma unroll
  for (int k = 0; k < 8; ++k) qa[k] = 0.f;
#pragma unroll
  for (int i = 0; i < 8; ++i) {
    const unsigned xp = i < 4 ? xa[i & 3] : xb[i & 3];
#pragma unroll
    for (int j = 0; j < 4; ++j) {
      qa[2 * j] = dot2(perm_lo(wqr[2 * i + 1][j], wqr[2 * i][j]), xp, qa[2 * j]);
      qa[2 * j + 1] = dot2(perm_hi(wqr[2 * i + 1][j], wqr[2 * i][j]), xp, qa[2 * j + 1]);
    }
    asm volatile("" : "+v"(qa[0]), "+v"(qa[1]), "+v"(qa[2]), "+v"(qa[3]), "+v"(qa[4]), "+v"(qa[5]), "+v"(qa[6]), "+v"(qa[7]));
    __builtin_amdgcn_sched_barrier(0);
  }
}
__device__ __forceinline__ void q_store(float (&qa)[8], float* lds, int wave, int r, int c) {
#pragma unroll
  for (int k = 0; k < 8; ++k) qa[k] = xrow_sum(qa[k]);
  if (r == 0) {
    f32x4* qd = reinterpret_cast<f32x4*>(lds + L_QP + wave * 128 + c * 8);
    qd[0] = f32x4{qa[0], qa[1], qa[2], qa[3]}; qd[1] = f32x4{qa[4], qa[5], qa[6], qa[7]};
  }
}

template <bool PF>
__device__ __forceinline__ void attn_step(u32x4 (&Kb)[8], u32x4 (&Vb)[8], __amdgpu_buffer_rsrc_t rsk, __amdgpu_buffer_rsrc_t rsv, unsigned so, unsigned vo,
                                          const u32x4& q, float& m, float& l, float (&acc)[8]) {
  float s[8];
#pragma unroll
  for (int i = 0; i < 8; ++i) {
    float d = dot2(Kb[i][0], q[0], 0.f);
    d = dot2(Kb[i][1], q[1], d); d = dot2(Kb[i][2], q[2], d); d = dot2(Kb[i][3], q[3], d);
    s[i] = d;
  }
  if (PF) {
#pragma unroll
    for (int i = 0; i < 8; ++i) Kb[i] = ldkv(rsk, vo, so, i);
  }
  __builtin_amdgcn_sched_barrier(0);
  constexpr float SCALE = 0.08838834764831845f, LOG2E = 1.4426950408889634f;
#pragma unroll
  for (int i = 0; i < 8; ++i) s[i] = row_sum(s[i]);
#pragma unroll
  for (int i = 0; i < 8; i += 2) {
    unsigned u = pack_bf16(s[i], s[i + 1]);
    u = pack_bf16(bf_lo(u) * SCALE, bf_hi(u) * SCALE);
    s[i] = bf_lo(u) * LOG2E; s[i + 1] = bf_hi(u) * LOG2E;
  }
  float mn = fmaxf(fmaxf(fmaxf(s[0], s[1]), fmaxf(s[2], s[3])), fmaxf(fmaxf(s[4], s[5]), fmaxf(s[6], s[7])));
  mn = fmaxf(mn, m);
  const float alpha = __builtin_amdgcn_exp2f(m - mn);
  m = mn;
  float ps = 0.f;
#pragma unroll
  for (int i = 0; i < 8; ++i) { s[i] = __builtin_amdgcn_exp2f(s[i] - mn); ps += s[i]; }
  l = fmaf(l, alpha, ps);
#pragma unroll
  for (int k = 0; k < 8; ++k) acc[k] *= alpha;
#pragma unroll
  for (int i = 0; i < 8; ++i) {
#pragma unroll
    for (int j = 0; j < 4; ++j) {
      acc[2 * j] = fmaf(s[i], bf_lo(Vb[i][j]), acc[2 * j]);
      acc[2 * j + 1] = fmaf(s[i], bf_hi(Vb[i][j]), acc[2 * j + 1]);
    }
  }
  if (PF) {
#pragma unroll
    for (int i = 0; i < 8; ++i) Vb[i] = ldkv(rsv, vo, so, i);
  }
  __builtin_amdgcn_sched_barrier(0);
}

__global__ void __launch_bounds__(NT, 4) fused_decode_kernel(Params p) {
  __shared__ __attribute__((aligned(16))) float lds[L_END];
  unsigned* ldsu = reinterpret_cast<unsigned*>(lds);
  const int bid = blockIdx.x, xcd = bid & 7, idx = bid >> 3;
  const int h = xcd * 4 + (idx >> 3), b = idx & 7;
  const int tid = threadIdx.x, lane = tid & 63, wave = __builtin_amdgcn_readfirstlane(tid >> 6);
  const int r = lane >> 4, c = lane & 15;
  const bool streamer = wave < NSW;

  const int k0 = (wave * 4 + r) * 16;
  const __amdgpu_buffer_rsrc_t rsq = __builtin_amdgcn_make_buffer_rsrc((void*)p.wq, 0, DM * HD * 2, 0x00020000);
  const unsigned wqs = (unsigned)(wave * 64 * HD + h * DH) * 2u;
  const unsigned wqo = (unsigned)(r * 16 * HD + c * 8) * 2u;

  if (streamer) {
    u32x4 wqr[16];
#pragma unroll
    for (int i = 0; i < 16; ++i) wqr[i] = __builtin_amdgcn_raw_buffer_load_b128(rsq, wqo, wqs + i * (HD * 2), 0);
    const size_t slab = (size_t)(b * NH + h) * SK * DH;
    const __amdgpu_buffer_rsrc_t rsk = __builtin_amdgcn_make_buffer_rsrc((void*)(p.kc + slab), 0, SK * DH * 2, 0x00020000);
    const __amdgpu_buffer_rsrc_t rsv = __builtin_amdgcn_make_buffer_rsrc((void*)(p.vc + slab), 0, SK * DH * 2, 0x00020000);
    unsigned so = (unsigned)wave * (CHK * DH * 2);
    const unsigned vo = (unsigned)lane * 16u;
    u32x4 Kb[8], Vb[8];
#pragma unroll
    for (int i = 0; i < 8; ++i) Kb[i] = ldkv(rsk, vo, so, i);
    __builtin_amdgcn_sched_barrier(0);
    __syncthreads();
    {
      float qa[8];
      q_partial(wqr, lds, k0, qa);
      __builtin_amdgcn_sched_barrier(0);
#pragma unroll
      for (int i = 0; i < 8; ++i) Vb[i] = ldkv(rsv, vo, so, i);
      __builtin_amdgcn_sched_barrier(0);
      q_store(qa, lds, wave, r, c);
    }
    __syncthreads();
    const unsigned tag = ldsu[L_CNT + 3];
    u32x4 q;
    {
      float qa[8];
#pragma unroll
      for (int k = 0; k < 8; ++k) qa[k] = 0.f;
#pragma unroll
      for (int w = 0; w < 4; ++w) {
        const f32x4* qs = reinterpret_cast<const f32x4*>(lds + L_QP + (r * 4 + w) * 128 + c * 8);
        const f32x4 a = qs[0], bb = qs[1];
#pragma unroll
        for (int k = 0; k < 4; ++k) { qa[k] += a[k]; qa[4 + k] += bb[k]; }
      }
#pragma unroll
      for (int k = 0; k < 8; ++k) qa[k] = xrow_sum(qa[k]);
#pragma unroll
      for (int j = 0; j < 4; ++j) q[j] = pack_bf16(qa[2 * j], qa[2 * j + 1]);
    }
    float m = -INFINITY, l = 0.f, acc[8];
#pragma unroll
    for (int k = 0; k < 8; ++k) acc[k] = 0.f;
    constexpr int STRIDE = NSW * CHK * DH * 2;
    for (int it = 0; it < NCH - 1; ++it) {
      so += STRIDE;
      attn_step<true>(Kb, Vb, rsk, rsv, so, vo, q, m, l, acc);
    }
    attn_step<false>(Kb, Vb, rsk, rsv, so, vo, q, m, l, acc);
    const float M = xrow_max(m);
    const float wt = __builtin_amdgcn_exp2f(m - M);
    l = xrow_sum(l * wt);
#pragma unroll
    for (int k = 0; k < 8; ++k) acc[k] = xrow_sum(acc[k] * wt);
    float* rec = lds + L_REC + wave * REC;
    if (r == 0) {
      f32x4* d = reinterpret_cast<f32x4*>(rec + 4 + c * 8);
      d[0] = f32x4{acc[0], acc[1], acc[2], acc[3]}; d[1] = f32x4{acc[4], acc[5], acc[6], acc[7]};
      if (c == 0) { rec[0] = M; rec[1] = l; }
    }
    unsigned arrived = 0;
    if (lane == 0) arrived = __hip_atomic_fetch_add(ldsu + L_CNT + 0, 1u, __ATOMIC_ACQ_REL, __HIP_MEMORY_SCOPE_WORKGROUP);
    arrived = __builtin_amdgcn_readfirstlane(arrived);
    if (arrived == NSW - 1) {
      __builtin_amdgcn_fence(__ATOMIC_ACQUIRE, "workgroup");
      float MM = -INFINITY;
#pragma unroll
      for (int w = 0; w < NSW; ++w) MM = fmaxf(MM, lds[L_REC + w * REC]);
      float L = 0.f, o0 = 0.f, o1 = 0.f;
#pragma unroll
      for (int w = 0; w < NSW; ++w) {
        const float* rw = lds + L_REC + w * REC;
        const float e = __builtin_amdgcn_exp2f(rw[0] - MM);
        const f32x2 a = *reinterpret_cast<const f32x2*>(rw + 4 + 2 * lane);
        L = fmaf(e, rw[1], L); o0 = fmaf(e, a[0], o0); o1 = fmaf(e, a[1], o1);
      }
      const float inv = 1.f / L;
      const unsigned pay = pack_bf16(o0 * inv, o1 * inv);
      gu64* og = (gu64*)(p.ws + WS_OG) + (size_t)b * (HD / 2) + h * (DH / 2) + lane;
      __hip_atomic_store(og, ((unsigned long long)tag << 32) | pay, RLX_AGENT);
      if (bid == 0) {
        gu64* all = (gu64*)(p.ws + WS_OG);
        for (unsigned spins = 0; spins < SPIN_MAX; ++spins) {
          bool ok = true;
#pragma unroll
          for (int j = 0; j < 4; ++j) ok &= (unsigned)(__hip_atomic_load(all + (size_t)(j * 64 + lane) * (DH / 2), RLX_AGENT) >> 32) == tag;
          if (__all(ok)) break;
          __builtin_amdgcn_s_sleep(2);
        }
        if (lane == 0) __hip_atomic_store((gu32*)(p.ws + WS_G), tag, RLX_AGENT);
      }
    }
    return;
  }

  const int hw = wave - NSW, ht = tid - NSW * 64;
  {
    const unsigned short* xr = p.x + hw * DM + lane * 16;
    const u32x4 x0 = ld16(xr), x1 = ld16(xr + 8);
    unsigned g0 = 0;
    g0 = __hip_atomic_load((gu32*)(p.ws + WS_G), RLX_AGENT);
    u32x4 wqr[16];
#pragma unroll
    for (int i = 0; i < 16; ++i) wqr[i] = __builtin_amdgcn_raw_buffer_load_b128(rsq, wqo, wqs + i * (HD * 2), 0);
    __builtin_amdgcn_sched_barrier(0);
    {
      float v[16];
      unpack16(x0, x1, v);
      const LnStat s1 = ln_stat(v);
      u32x4 y0, y1;
#pragma unroll
      for (int j = 0; j < 4; ++j) {
        y0[j] = pack_bf16(ln_apply(v[2 * j], s1), ln_apply(v[2 * j + 1], s1));
        y1[j] = pack_bf16(ln_apply(v[8 + 2 * j], s1), ln_apply(v[8 + 2 * j + 1], s1));
      }
      if (hw == b) { u32x4* xd = reinterpret_cast<u32x4*>(lds + L_XN + lane * 8); xd[0] = y0; xd[1] = y1; }
      unpack16(y0, y1, v);
      const LnStat s2 = ln_stat(v);
#pragma unroll
      for (int j = 0; j < 4; ++j) {
        y0[j] = pack_bf16(ln_apply(v[2 * j], s2), ln_apply(v[2 * j + 1], s2));
        y1[j] = pack_bf16(ln_apply(v[8 + 2 * j], s2), ln_apply(v[8 + 2 * j + 1], s2));
      }
      u32x4* yd = reinterpret_cast<u32x4*>(lds + L_Y + hw * 512 + lane * 8);
      yd[0] = y0; yd[1] = y1;
      if (ht == 0) { ldsu[L_CNT + 3] = g0 + 1u; ldsu[L_CNT + 0] = 0u; ldsu[L_CNT + 1] = 0u; ldsu[L_CNT + 2] = 0u; }
    }
    __syncthreads();
    float qa[8];
    q_partial(wqr, lds, k0, qa);
    q_store(qa, lds, wave, r, c);
    __syncthreads();
  }
  const unsigned tag = ldsu[L_CNT + 3];
  const int bg = idx >> 4, cg = xcd * 16 + (idx & 15);
  if (idx < 28) {
    const int f0 = xcd * 448 + idx * 16;
    const int kr = (ht >> 1) * 4;
    const unsigned short* wp = p.w1 + (size_t)kr * FF + f0 + (ht & 1) * 8;
    u32x4 w[4];
#pragma unroll
    for (int j = 0; j < 4; ++j) w[j] = ld16(wp + (size_t)j * FF);
    const int fo = f0 + (lane & 1) * 8 + ((lane >> 1) & 3) * 2;
    const unsigned b1p = *reinterpret_cast<const unsigned*>(p.b1 + fo);
    float a[64];
#pragma unroll
    for (int bb = 0; bb < 8; ++bb) {
      const u32x2 y = *reinterpret_cast<const u32x2*>(lds + L_Y + bb * 512 + (kr >> 1));
#pragma unroll
      for (int j = 0; j < 4; ++j) {
        a[bb * 8 + 2 * j] = dot2(perm_lo(w[3][j], w[2][j]), y[1], dot2(perm_lo(w[1][j], w[0][j]), y[0], 0.f));
        a[bb * 8 + 2 * j + 1] = dot2(perm_hi(w[3][j], w[2][j]), y[1], dot2(perm_hi(w[1][j], w[0][j]), y[0], 0.f));
      }
    }
    rs_step<64, 32>(a, lane); rs_step<32, 16>(a, lane); rs_step<16, 8>(a, lane); rs_step<8, 4>(a, lane); rs_step<4, 2>(a, lane);
    *reinterpret_cast<f32x2*>(lds + L_HP + hw * 128 + lane * 2) = f32x2{a[0], a[1]};
    unsigned arrived = 0;
    if (lane == 0) arrived = __hip_atomic_fetch_add(ldsu + L_CNT + 1, 1u, __ATOMIC_ACQ_REL, __HIP_MEMORY_SCOPE_WORKGROUP);
    arrived = __builtin_amdgcn_readfirstlane(arrived);
    if (arrived == NHW - 1) {
      __builtin_amdgcn_fence(__ATOMIC_ACQUIRE, "workgroup");
      float t0 = 0.f, t1 = 0.f;
#pragma unroll
      for (int w2 = 0; w2 < NHW; ++w2) { const f32x2 t = *reinterpret_cast<const f32x2*>(lds + L_HP + w2 * 128 + lane * 2); t0 += t[0]; t1 += t[1]; }
      t0 = bfr(bfr(t0) + bf_lo(b1p)); t1 = bfr(bfr(t1) + bf_hi(b1p));
      const float g0 = 0.5f * t0 * (1.f + tanhf(0.7978845608028654f * (t0 + 0.044715f * t0 * t0 * t0)));
      const float g1 = 0.5f * t1 * (1.f + tanhf(0.7978845608028654f * (t1 + 0.044715f * t1 * t1 * t1)));
      gu64* hg = (gu64*)(p.ws + WS_HG) + (size_t)(lane >> 3) * (FF / 2) + (fo >> 1);
      __hip_atomic_store(hg, ((unsigned long long)tag << 32) | pack_bf16(g0, g1), RLX_AGENT);
    }
  }
  float acc[32];
#pragma unroll
  for (int k = 0; k < 32; ++k) acc[k] = 0.f;
  const __amdgpu_buffer_rsrc_t rs = __builtin_amdgcn_make_buffer_rsrc(p.ws, 0, (int)WS_END, 0x00020000);
  unsigned wp[32];
  if (hw < 7) {
    u32x4 w[8];
    const unsigned short* s = p.w2 + (size_t)(ht * 8) * DM + cg * 8;
#pragma unroll
    for (int j = 0; j < 8; ++j) w[j] = ld16(s + j * DM);
    pair_rows(w, wp);
    gather_acc(rs, (unsigned)(WS_HG + ((size_t)(bg * 4) * (FF / 2) + ht * 4) * 8), (FF / 2) * 8, tag, wp, acc, true);
  }
  {
    u32x4 w[8];
    const unsigned short* s = p.wo + (size_t)(ht * 8) * DM + cg * 8;
#pragma unroll
    for (int j = 0; j < 8; ++j) w[j] = ld16(s + j * DM);
    pair_rows(w, wp);
#pragma unroll
    for (int k = 0; k < 32; ++k) asm volatile("" : "+v"(wp[k]));
  }
  const int ob = bg * 4 + ((lane >> 3) & 3), on = cg * 8 + (lane & 7);
  const float resid = bf_lo(p.x[ob * DM + on]) + bf_lo(p.b2[on]);
  for (unsigned spins = 0; spins < 50u * SPIN_MAX; ++spins) {
    if (__hip_atomic_load(ldsu + L_CNT + 0, __ATOMIC_RELAXED, __HIP_MEMORY_SCOPE_WORKGROUP) >= (unsigned)NSW) break;
    __builtin_amdgcn_s_sleep(8);
  }
  gather_acc(rs, (unsigned)(WS_OG + ((size_t)(bg * 4) * (HD / 2) + ht * 4) * 8), (HD / 2) * 8, tag, wp, acc, true);
  rs_step<32, 32>(acc, lane); rs_step<16, 16>(acc, lane); rs_step<8, 8>(acc, lane); rs_step<4, 4>(acc, lane); rs_step<2, 2>(acc, lane);
  acc[0] += dpp<XOR1>(acc[0]);
  if ((lane & 1) == 0) lds[L_FP + hw * 32 + (lane >> 1)] = acc[0];
  unsigned arrived = 0;
  if (lane == 0) arrived = __hip_atomic_fetch_add(ldsu + L_CNT + 2, 1u, __ATOMIC_ACQ_REL, __HIP_MEMORY_SCOPE_WORKGROUP);
  arrived = __builtin_amdgcn_readfirstlane(arrived);
  if (arrived == NHW - 1) {
    __builtin_amdgcn_fence(__ATOMIC_ACQUIRE, "workgroup");
    if (lane < 32) {
      float t = resid;
#pragma unroll
      for (int w2 = 0; w2 < NHW; ++w2) t += lds[L_FP + w2 * 32 + lane];
      p.out[ob * DM + on] = (unsigned short)(pack_bf16(t, 0.f) & 0xffffu);
    }
  }
}

}

extern "C" void kernel_launch(void* const* d_in, const int* in_sizes, int n_in, void* d_out, int out_size, void* d_ws,
                              size_t ws_size, hipStream_t stream) {
  Params p;
  p.x = (const unsigned short*)d_in[0];
  p.kc = (const unsigned short*)d_in[1];
  p.vc = (const unsigned short*)d_in[2];
  p.wq = (const unsigned short*)d_in[3];
  p.wo = (const unsigned short*)d_in[4];
  p.w1 = (const unsigned short*)d_in[6];
  p.b1 = (const unsigned short*)d_in[7];
  p.w2 = (const unsigned short*)d_in[8];
  p.b2 = (const unsigned short*)d_in[9];
  p.out = (unsigned short*)d_out;
  p.ws = (unsigned char*)d_ws;
  fused_decode_kernel<<<dim3(NB * NH), dim3(NT), 0, stream>>>(p);
}
```

```cpp
#include <hip/hip_runtime.h>
#include <hip/hip_bf16.h>
#include <stdint.h>

#define MIMIC 1
#define USE_MEMSET 0

namespace {

constexpr int NB = 8, DM = 1024, NH = 32, SK = 4096, DH = 128, FF = 3584, HD = NH * DH;
#define CFG_NSW 8
#define CFG_NL 2
#define CFG_XB 3
#define CFG_DELAY 0
#define CFG_ROT 0
#define PACE_END 7700
#define PACE_BEG 400
#define CFG_WIN 1
#define TWIN 2
#define PSLEEP 4
#define WAUX 2
constexpr int NSW = CFG_NSW, NHW = 8, NT = (NSW + NHW) * 64;
constexpr int NL = CFG_NL;
constexpr int CHK = NL * 4;
constexpr int NCH = SK / CHK / NSW;
constexpr unsigned SPIN_MAX = 200000u;

constexpr int NG = FF / 128;
constexpr size_t WS_G = 0;
constexpr size_t WS_OG = 4096;
constexpr size_t WS_HG = WS_OG + (size_t)NH * NB * 64 * 8;
constexpr size_t WS_QG = WS_HG + (size_t)NG * NB * 64 * 8;
constexpr size_t WS_UG = WS_QG + (size_t)NH * NB * NB * 128 * 8;
constexpr size_t WS_PG = WS_UG + (size_t)NG * NB * NB * 128 * 8;
constexpr size_t WS_END = WS_PG + (size_t)NB * DM * NH * 8;

typedef __attribute__((ext_vector_type(2))) __bf16 bf16x2;
typedef __attribute__((ext_vector_type(2))) float f32x2;
typedef __attribute__((ext_vector_type(4))) float f32x4;
typedef __attribute__((ext_vector_type(4))) unsigned u32x4;
typedef __attribute__((ext_vector_type(2))) unsigned u32x2;
typedef __attribute__((ext_vector_type(8))) __bf16 bf16x8;
typedef __attribute__((address_space(1))) unsigned gu32;
typedef __attribute__((address_space(1))) unsigned long long gu64;
#define RLX_AGENT __ATOMIC_RELAXED, __HIP_MEMORY_SCOPE_AGENT

__device__ __forceinline__ float dot2(unsigned a, unsigned b, float acc) {
  return __builtin_amdgcn_fdot2_f32_bf16(__builtin_bit_cast(bf16x2, a), __builtin_bit_cast(bf16x2, b), acc, false);
}
__device__ __forceinline__ unsigned pack_bf16(float lo, float hi) {
  f32x2 v = {lo, hi};
  return __builtin_bit_cast(unsigned, __builtin_convertvector(v, bf16x2));
}
__device__ __forceinline__ float bf_lo(unsigned u) { return __builtin_bit_cast(float, u << 16); }
__device__ __forceinline__ float bf_hi(unsigned u) { return __builtin_bit_cast(float, u & 0xffff0000u); }
__device__ __forceinline__ float bfr(float x) { return bf_lo(pack_bf16(x, 0.f)); }
template <int CTRL> __device__ __forceinline__ float dpp(float x) {
  return __builtin_bit_cast(float, __builtin_amdgcn_mov_dpp(__builtin_bit_cast(int, x), CTRL, 0xf, 0xf, true));
}
constexpr int XOR1 = 0xB1, XOR2 = 0x4E, XOR7 = 0x141, XOR8 = 0x128;
__device__ __forceinline__ float row_sum(float x) {
  x += dpp<XOR1>(x); x += dpp<XOR2>(x); x += dpp<XOR7>(x); x += dpp<XOR8>(x); return x;
}
__device__ __forceinline__ float xrow_sum(float x) {
  auto s = __builtin_amdgcn_permlane16_swap(__float_as_uint(x), __float_as_uint(x), false, false);
  x = __uint_as_float(s[0]) + __uint_as_float(s[1]);
  auto t = __builtin_amdgcn_permlane32_swap(__float_as_uint(x), __float_as_uint(x), false, false);
  return __uint_as_float(t[0]) + __uint_as_float(t[1]);
}
__device__ __forceinline__ float xrow_max(float x) {
  auto s = __builtin_amdgcn_permlane16_swap(__float_as_uint(x), __float_as_uint(x), false, false);
  x = fmaxf(__uint_as_float(s[0]), __uint_as_float(s[1]));
  auto t = __builtin_amdgcn_permlane32_swap(__float_as_uint(x), __float_as_uint(x), false, false);
  return fmaxf(__uint_as_float(t[0]), __uint_as_float(t[1]));
}
__device__ __forceinline__ float wave_sum(float x) { return xrow_sum(row_sum(x)); }
__device__ __forceinline__ unsigned perm_lo(unsigned hi_src, unsigned lo_src) { return __builtin_amdgcn_perm(hi_src, lo_src, 0x05040100u); }
__device__ __forceinline__ unsigned perm_hi(unsigned hi_src, unsigned lo_src) { return __builtin_amdgcn_perm(hi_src, lo_src, 0x07060302u); }
__device__ __forceinline__ u32x4 ld16(const void* p) { return *reinterpret_cast<const u32x4*>(p); }
__device__ __forceinline__ u32x4 ld16nt(const void* p) { return __builtin_nontemporal_load(reinterpret_cast<const u32x4*>(p)); }

__device__ __forceinline__ u32x4 ldkv(__amdgpu_buffer_rsrc_t rs, unsigned vo, unsigned so, int i) {
  return __builtin_amdgcn_raw_buffer_load_b128(rs, vo + (i & 3) * 1024, so + (i >> 2) * 4096, 2);
}

struct LnStat { float sub, den; };
__device__ __forceinline__ LnStat ln_stat(const float (&v)[16]) {
  float s = 0.f;
#pragma unroll
  for (int i = 0; i < 16; ++i) s += v[i];
  const float mean = wave_sum(s) * (1.f / DM);
  float q = 0.f;
#pragma unroll
  for (int i = 0; i < 16; ++i) { const float d = v[i] - mean; q = fmaf(d, d, q); }
  const float var = wave_sum(q) * (1.f / DM);
  LnStat st;
  st.sub = bfr(mean);
  st.den = bfr(sqrtf(bfr(bfr(var) + 1e-5f)));
  return st;
}
__device__ __forceinline__ float ln_apply(float x, LnStat st) {
  return bfr(x - st.sub) / st.den;
}
__device__ __forceinline__ void unpack16(u32x4 a, u32x4 b, float (&v)[16]) {
#pragma unroll
  for (int j = 0; j < 4; ++j) { v[2 * j] = bf_lo(a[j]); v[2 * j + 1] = bf_hi(a[j]); v[8 + 2 * j] = bf_lo(b[j]); v[8 + 2 * j + 1] = bf_hi(b[j]); }
}
#define STAMP(i) do {} while (0)

struct Params {
  const unsigned short* x; const unsigned short* kc; const unsigned short* vc; const unsigned short* wq; const unsigned short* wo;
  const unsigned short* w1; const unsigned short* b1; const unsigned short* w2; const unsigned short* b2;
  unsigned short* out; unsigned char* ws;
};

constexpr int TROW = 68;
constexpr int TILE = 128 * TROW;
constexpr int L_T = 0;
constexpr int L_Q = L_T + 4 * TILE;
constexpr int L_XN = L_Q + 64;
constexpr int L_Y = L_XN + 512;
constexpr int L_H = L_Y + 512;
constexpr int L_D = L_H + 512;
constexpr int L_REC = L_D + 2048;
constexpr int REC = 132;
constexpr int L_CNT = L_REC + NSW * REC;
constexpr int L_END = L_CNT + 16;
enum { C_STREAM = 0, C_XN, C_Y, C_TAG, C_Q, C_T0, C_T1, C_T2, C_T3, C_H, C_D, C_ROT, C_O };
constexpr unsigned OOB = 0xfffffff0u;

template <int N> __device__ __forceinline__ void window() {
  __builtin_amdgcn_sched_barrier(0);
  asm volatile("s_waitcnt vmcnt(%0)" ::"i"(N) : "memory");
  __builtin_amdgcn_sched_barrier(0);
}
template <int ROWB, bool WIN>
__device__ __forceinline__ void tile_load(__amdgpu_buffer_rsrc_t rw, unsigned off, u32x4 (&t)[8]) {
#pragma unroll
  for (int i = 0; i < 8; ++i) {
    if (WIN && (i & 1) == 0) window<TWIN>();
    t[i] = __builtin_amdgcn_raw_buffer_load_b128(rw, off, i * 4 * ROWB, WAUX);
  }
}
__device__ __forceinline__ void tile_store(float* tile, int gw, int r, int c, const u32x4 (&t)[8]) {
#pragma unroll
  for (int i = 0; i < 8; ++i) *reinterpret_cast<u32x4*>(tile + (gw * 32 + i * 4 + r) * TROW + c * 4) = t[i];
}
__device__ __forceinline__ void tile_frags(const float* tile, int gw, int r, int c, unsigned (&raw)[32]) {
#pragma unroll
  for (int i = 0; i < 32; ++i) raw[i] = __float_as_uint(tile[((i >> 3) * 32 + r * 8 + (i & 7)) * TROW + gw * 16 + c]);
}
__device__ __forceinline__ void act_frags(const float* act, int r, int c, u32x4 (&af)[4]) {
#pragma unroll
  for (int kk = 0; kk < 4; ++kk) {
    af[kk] = *reinterpret_cast<const u32x4*>(act + (c & 7) * 64 + kk * 16 + r * 4);
    if (c >= 8) af[kk] = u32x4{0u, 0u, 0u, 0u};
  }
}
__device__ __forceinline__ void pair_tile(const unsigned (&raw)[32], u32x4 (&bf)[8]) {
#pragma unroll
  for (int kk = 0; kk < 4; ++kk)
#pragma unroll
    for (int e = 0; e < 4; ++e) {
      bf[kk * 2][e] = perm_lo(raw[kk * 8 + 2 * e + 1], raw[kk * 8 + 2 * e]);
      bf[kk * 2 + 1][e] = perm_hi(raw[kk * 8 + 2 * e + 1], raw[kk * 8 + 2 * e]);
    }
}
__device__ __forceinline__ void mfma_tile(const u32x4 (&af)[4], const u32x4 (&bf)[8], f32x4& d0, f32x4& d1) {
#pragma unroll
  for (int kk = 0; kk < 4; ++kk) {
    d0 = __builtin_amdgcn_mfma_f32_16x16x32_bf16(__builtin_bit_cast(bf16x8, af[kk]), __builtin_bit_cast(bf16x8, bf[kk * 2]), d0, 0, 0, 0);
    d1 = __builtin_amdgcn_mfma_f32_16x16x32_bf16(__builtin_bit_cast(bf16x8, af[kk]), __builtin_bit_cast(bf16x8, bf[kk * 2 + 1]), d1, 0, 0, 0);
  }
}
template <int SLEEP, bool NARROW>
__device__ __forceinline__ void sweep8(__amdgpu_buffer_rsrc_t rs, unsigned off0, unsigned shi, unsigned slo, unsigned tag,
                                       unsigned pend, unsigned (&v)[16]) {
#pragma unroll
  for (int j = 0; j < 16; ++j) v[j] = 0u;
  unsigned act = pend;
  for (unsigned spins = 0; spins < SPIN_MAX; ++spins) {
    u32x4 g[8];
#pragma unroll
    for (int j = 0; j < 8; ++j) {
      const unsigned off = ((act >> j) & 1u) ? off0 + (j >> 1) * shi + (j & 1) * slo : OOB;
      g[j] = __builtin_amdgcn_raw_buffer_load_b128(rs, off, 0, 16);
    }
    __builtin_amdgcn_sched_barrier(0);
    const unsigned before = pend;
#pragma unroll
    for (int j = 0; j < 8; ++j) {
      const bool ok = ((act >> j) & 1u) && g[j][1] == tag && g[j][3] == tag;
      v[2 * j] = ok ? g[j][0] : v[2 * j];
      v[2 * j + 1] = ok ? g[j][2] : v[2 * j + 1];
      pend = ok ? (pend & ~(1u << j)) : pend;
    }
    if (!__any(pend != 0u)) break;
    if (NARROW) {
      const bool was_narrow_hit = __any(pend != before && act != before);
      act = was_narrow_hit ? pend : (pend & (0u - pend));
      if (!was_narrow_hit) __builtin_amdgcn_s_sleep(SLEEP);
    } else {
      act = pend;
      __builtin_amdgcn_s_sleep(SLEEP);
    }
  }
}
__device__ __forceinline__ void put_granule(gu64* g, unsigned tag, unsigned val) {
  __hip_atomic_store(g, ((unsigned long long)tag << 32) | val, RLX_AGENT);
}
__device__ __forceinline__ void put_rows(__amdgpu_buffer_rsrc_t rs, unsigned off, unsigned rstride, unsigned tag, int r,
                                         const f32x4& d0, const f32x4& d1) {
  if (r < 2) {
#pragma unroll
    for (int i = 0; i < 4; ++i)
      __builtin_amdgcn_raw_buffer_store_b128(u32x4{__float_as_uint(d0[i]), tag, __float_as_uint(d1[i]), tag}, rs,
                                             off + (r * 4 + i) * rstride, 0, 16);
  }
}
__device__ __forceinline__ void lds_arrive(unsigned* cnt, int lane) {
  asm volatile("s_waitcnt lgkmcnt(0)" ::: "memory");
  if (lane == 0) __hip_atomic_fetch_add(cnt, 1u, __ATOMIC_RELAXED, __HIP_MEMORY_SCOPE_WORKGROUP);
}
template <int SLEEP>
__device__ __forceinline__ void lds_wait(unsigned* cnt, unsigned n) {
  for (unsigned spins = 0; spins < 50u * SPIN_MAX; ++spins) {
    if (__hip_atomic_load(cnt, __ATOMIC_RELAXED, __HIP_MEMORY_SCOPE_WORKGROUP) >= n) break;
    __builtin_amdgcn_s_sleep(SLEEP);
  }
  asm volatile("" ::: "memory");
}

template <bool PF>
__device__ __forceinline__ void attn_step(u32x4 (&Kb)[NL], u32x4 (&Vb)[NL], __amdgpu_buffer_rsrc_t rsk, __amdgpu_buffer_rsrc_t rsv, unsigned so, unsigned vo,
                                          const u32x4& q, float& m, float& l, float (&acc)[8]) {
  float s[NL];
#pragma unroll
  for (int i = 0; i < NL; ++i) {
    float d = dot2(Kb[i][0], q[0], 0.f);
    d = dot2(Kb[i][1], q[1], d); d = dot2(Kb[i][2], q[2], d); d = dot2(Kb[i][3], q[3], d);
    s[i] = d;
  }
  if (PF) {
#pragma unroll
    for (int i = 0; i < NL; ++i) Kb[i] = ldkv(rsk, vo, so, i);
  }
  __builtin_amdgcn_sched_barrier(0);
  constexpr float SCALE = 0.08838834764831845f, LOG2E = 1.4426950408889634f;
#pragma unroll
  for (int i = 0; i < NL; ++i) s[i] = row_sum(s[i]);
#pragma unroll
  for (int i = 0; i < NL; i += 2) {
    unsigned u = pack_bf16(s[i], s[i + 1]);
    u = pack_bf16(bf_lo(u) * SCALE, bf_hi(u) * SCALE);
    s[i] = bf_lo(u) * LOG2E; s[i + 1] = bf_hi(u) * LOG2E;
  }
  float mn = m;
#pragma unroll
  for (int i = 0; i < NL; ++i) mn = fmaxf(mn, s[i]);
  const float alpha = __builtin_amdgcn_exp2f(m - mn);
  m = mn;
  float ps = 0.f;
#pragma unroll
  for (int i = 0; i < NL; ++i) { s[i] = __builtin_amdgcn_exp2f(s[i] - mn); ps += s[i]; }
  l = fmaf(l, alpha, ps);
#pragma unroll
  for (int k = 0; k < 8; ++k) acc[k] *= alpha;
#pragma unroll
  for (int i = 0; i < NL; ++i) {
#pragma unroll
    for (int j = 0; j < 4; ++j) {
      acc[2 * j] = fmaf(s[i], bf_lo(Vb[i][j]), acc[2 * j]);
      acc[2 * j + 1] = fmaf(s[i], bf_hi(Vb[i][j]), acc[2 * j + 1]);
    }
  }
  if (PF) {
#pragma unroll
    for (int i = 0; i < NL; ++i) Vb[i] = ldkv(rsv, vo, so, i);
  }
  __builtin_amdgcn_sched_barrier(0);
}

__device__ __forceinline__ void q_phase(const Params& p, float* lds, __amdgpu_buffer_rsrc_t rs, int h, int b, int hw, int lane) {
  unsigned* cnt = reinterpret_cast<unsigned*>(lds) + L_CNT;
  const int r = lane >> 4, c = lane & 15;
  u32x4 bf[8], af[4];
  unsigned raw[32];
  f32x4 d0 = {0.f, 0.f, 0.f, 0.f}, d1 = {0.f, 0.f, 0.f, 0.f};
  lds_wait<1>(cnt + C_T0, 4);
  tile_frags(lds + L_T, hw, r, c, raw);
  pair_tile(raw, bf);
  lds_wait<1>(cnt + C_XN, NHW);
  const unsigned tag = cnt[C_TAG];
  act_frags(lds + L_XN, r, c, af);
  mfma_tile(af, bf, d0, d1);
  const unsigned pbase = (unsigned)WS_QG + (unsigned)h * (64 * 1024);
  put_rows(rs, pbase + (unsigned)(b * 128 + hw * 32 + c * 2) * 8u, 8 * 1024, tag, r, d0, d1);
  if (hw == 0) {
    unsigned gr[16];
    sweep8<1, false>(rs, pbase + (unsigned)b * (8 * 1024) + (unsigned)lane * 16u, 2048, 1024, tag, 0xffu, gr);
    float t0 = 0.f, t1 = 0.f;
#pragma unroll
    for (int j = 0; j < 8; ++j) { t0 += __uint_as_float(gr[2 * j]); t1 += __uint_as_float(gr[2 * j + 1]); }
    reinterpret_cast<unsigned*>(lds)[L_Q + lane] = pack_bf16(t0, t1);
    lds_arrive(cnt + C_Q, lane);
  }
}

__global__ void __launch_bounds__(NT, NT / 256) fused_decode_kernel(Params p) {
  __shared__ __attribute__((aligned(16))) float lds[L_END];
  unsigned* ldsu = reinterpret_cast<unsigned*>(lds);
  const int bid = blockIdx.x, xcd = bid & 7, idx = bid >> 3;
  const int h = xcd * 4 + (idx >> 3), b = idx & 7;
  const int tid = threadIdx.x, lane = tid & 63, wave = __builtin_amdgcn_readfirstlane(tid >> 6);
  const int r = lane >> 4, c = lane & 15;
  const bool streamer = wave >= NHW;
  if (wave == 0) STAMP(0);

  if (streamer) {
    const int sw = wave - NHW;
    const size_t slab = (size_t)(b * NH + h) * SK * DH;
    const __amdgpu_buffer_rsrc_t rsk = __builtin_amdgcn_make_buffer_rsrc((void*)(p.kc + slab), 0, SK * DH * 2, 0x00020000);
    const __amdgpu_buffer_rsrc_t rsv = __builtin_amdgcn_make_buffer_rsrc((void*)(p.vc + slab), 0, SK * DH * 2, 0x00020000);
    unsigned so = (unsigned)sw * (CHK * DH * 2);
    const unsigned vo = (unsigned)lane * 16u;
    constexpr int STRIDE = NSW * CHK * DH * 2;
    constexpr int XB = CFG_XB;
    u32x4 Kb[NL], Vb[NL], Kx[XB > 0 ? XB : 1][NL], Vx[XB > 0 ? XB : 1][NL];
    const unsigned t0 = (unsigned)__builtin_amdgcn_s_memrealtime();
    __syncthreads();
    if (CFG_DELAY) __builtin_amdgcn_s_sleep(CFG_DELAY);
#pragma unroll
    for (int i = 0; i < NL; ++i) Kb[i] = ldkv(rsk, vo, so, i);
#pragma unroll
    for (int i = 0; i < NL; ++i) Vb[i] = ldkv(rsv, vo, so, i);
#pragma unroll
    for (int x = 0; x < XB; ++x) {
      window<NL>();
#pragma unroll
      for (int i = 0; i < NL; ++i) Kx[x][i] = ldkv(rsk, vo, so + (NCH - 1 - x) * STRIDE, i);
      window<NL>();
#pragma unroll
      for (int i = 0; i < NL; ++i) Vx[x][i] = ldkv(rsv, vo, so + (NCH - 1 - x) * STRIDE, i);
    }
    __builtin_amdgcn_sched_barrier(0);
    lds_wait<2>(ldsu + L_CNT + C_Q, 1);
    if (sw == 0) STAMP(2);
    const unsigned tag = ldsu[L_CNT + C_TAG];
    const u32x4 q = *reinterpret_cast<const u32x4*>(lds + L_Q + c * 4);
    float m = -INFINITY, l = 0.f, acc[8];
#pragma unroll
    for (int k = 0; k < 8; ++k) acc[k] = 0.f;
    if (XB > 0) {
      so += STRIDE;
      attn_step<true>(Kb, Vb, rsk, rsv, so, vo, q, m, l, acc);
#pragma unroll
      for (int x = 0; x < XB; ++x) attn_step<false>(Kx[x], Vx[x], rsk, rsv, so, vo, q, m, l, acc);
    }
    for (int it = (XB > 0 ? 1 : 0); it < NCH - 1 - XB; ++it) {
      so += STRIDE;
      {
        const unsigned due = PACE_BEG + (unsigned)((it + XB) * ((PACE_END - PACE_BEG) * 16 / NCH)) / 16u;
        while ((unsigned)__builtin_amdgcn_s_memrealtime() - t0 < due) __builtin_amdgcn_s_sleep(1);
      }
      attn_step<true>(Kb, Vb, rsk, rsv, so, vo, q, m, l, acc);
    }
    attn_step<false>(Kb, Vb, rsk, rsv, so, vo, q, m, l, acc);
    STAMP(16 + sw);
    const float M = xrow_max(m);
    const float wt = __builtin_amdgcn_exp2f(m - M);
    l = xrow_sum(l * wt);
#pragma unroll
    for (int k = 0; k < 8; ++k) acc[k] = xrow_sum(acc[k] * wt);
    float* rec = lds + L_REC + sw * REC;
    if (r == 0) {
      f32x4* d = reinterpret_cast<f32x4*>(rec + 4 + c * 8);
      d[0] = f32x4{acc[0], acc[1], acc[2], acc[3]}; d[1] = f32x4{acc[4], acc[5], acc[6], acc[7]};
      if (c == 0) { rec[0] = M; rec[1] = l; }
    }
    unsigned arrived = 0;
    if (lane == 0) arrived = __hip_atomic_fetch_add(ldsu + L_CNT + C_STREAM, 1u, __ATOMIC_ACQ_REL, __HIP_MEMORY_SCOPE_WORKGROUP);
    arrived = __builtin_amdgcn_readfirstlane(arrived);
    if (arrived == NSW - 1) {
      __builtin_amdgcn_fence(__ATOMIC_ACQUIRE, "workgroup");
      float MM = -INFINITY;
#pragma unroll
      for (int w = 0; w < NSW; ++w) MM = fmaxf(MM, lds[L_REC + w * REC]);
      float L = 0.f, o0 = 0.f, o1 = 0.f;
#pragma unroll
      for (int w = 0; w < NSW; ++w) {
        const float* rw = lds + L_REC + w * REC;
        const float e = __builtin_amdgcn_exp2f(rw[0] - MM);
        const f32x2 a = *reinterpret_cast<const f32x2*>(rw + 4 + 2 * lane);
        L = fmaf(e, rw[1], L); o0 = fmaf(e, a[0], o0); o1 = fmaf(e, a[1], o1);
      }
      const float inv = 1.f / L;
      const unsigned pay = pack_bf16(o0 * inv, o1 * inv);
      gu64* og = (gu64*)(p.ws + WS_OG) + (size_t)(h * NB + b) * 64 + lane;
      __hip_atomic_store(og, ((unsigned long long)tag << 32) | pay, RLX_AGENT);
      STAMP(3);
    }
    return;
  }

  const int hw = wave, gw = wave & 3;
  const bool grpA = hw < 4;
  __builtin_amdgcn_s_setprio(3);
  unsigned* cnt = ldsu + L_CNT;
  const __amdgpu_buffer_rsrc_t rs = __builtin_amdgcn_make_buffer_rsrc(p.ws, 0, (int)WS_END, 0x00020000);
  const bool mlp = idx < 24 || ((idx < 28) == ((xcd & 1) == 0));
  const int g = idx < 24 ? xcd * 3 + (idx >> 3) : 24 + (xcd >> 1);

  const unsigned short* xr = p.x + hw * DM + lane * 16;
  const u32x4 x0 = ld16(xr), x1 = ld16(xr + 8);
  unsigned g0 = 0;
  g0 = __hip_atomic_load((gu32*)(p.ws + WS_G), RLX_AGENT);
  u32x4 ta[8], tb[8];
  if (grpA) {
    const __amdgpu_buffer_rsrc_t r0 = __builtin_amdgcn_make_buffer_rsrc((void*)p.wq, 0, DM * HD * 2, 0x00020000);
    tile_load<HD * 2, false>(r0, (unsigned)((b * 128 + gw * 32 + r) * HD + h * 128 + c * 8) * 2u, ta);
  }
  if (tid < 16) cnt[tid] = 0u;
  __builtin_amdgcn_sched_barrier(0);
  __syncthreads();
  {
    float v[16];
    unpack16(x0, x1, v);
    const LnStat s1 = ln_stat(v);
    u32x4 y0, y1;
#pragma unroll
    for (int j = 0; j < 4; ++j) {
      y0[j] = pack_bf16(ln_apply(v[2 * j], s1), ln_apply(v[2 * j + 1], s1));
      y1[j] = pack_bf16(ln_apply(v[8 + 2 * j], s1), ln_apply(v[8 + 2 * j + 1], s1));
    }
    if ((lane >> 3) == b) {
      u32x4* xd = reinterpret_cast<u32x4*>(lds + L_XN + hw * 64 + (lane & 7) * 8);
      xd[0] = y0; xd[1] = y1;
    }
    if (tid == 0) cnt[C_TAG] = g0 + 1u;
    lds_arrive(cnt + C_XN, lane);
    if (grpA) {
      tile_store(lds + L_T, gw, r, c, ta);
      lds_arrive(cnt + C_T0, lane);
      q_phase(p, lds, rs, h, b, hw, lane);
      const __amdgpu_buffer_rsrc_t r1 = __builtin_amdgcn_make_buffer_rsrc((void*)p.w2, 0, mlp ? FF * DM * 2 : 0, 0x00020000);
      tile_load<DM * 2, true>(r1, (unsigned)((g * 128 + gw * 32 + r) * DM + b * 128 + c * 8) * 2u, tb);
    }
    __builtin_amdgcn_sched_barrier(0);
    unpack16(y0, y1, v);
    const LnStat s2 = ln_stat(v);
#pragma unroll
    for (int j = 0; j < 4; ++j) {
      y0[j] = pack_bf16(ln_apply(v[2 * j], s2), ln_apply(v[2 * j + 1], s2));
      y1[j] = pack_bf16(ln_apply(v[8 + 2 * j], s2), ln_apply(v[8 + 2 * j + 1], s2));
    }
    if ((lane >> 3) == b) {
      u32x4* yd = reinterpret_cast<u32x4*>(lds + L_Y + hw * 64 + (lane & 7) * 8);
      yd[0] = y0; yd[1] = y1;
    }
    lds_arrive(cnt + C_Y, lane);
    if (!grpA) {
      const __amdgpu_buffer_rsrc_t r0 = __builtin_amdgcn_make_buffer_rsrc((void*)p.w1, 0, mlp ? DM * FF * 2 : 0, 0x00020000);
      tile_load<FF * 2, true>(r0, (unsigned)((b * 128 + gw * 32 + r) * FF + g * 128 + c * 8) * 2u, ta);
      const __amdgpu_buffer_rsrc_t r1 = __builtin_amdgcn_make_buffer_rsrc((void*)p.wo, 0, HD * DM * 2, 0x00020000);
      tile_load<DM * 2, true>(r1, (unsigned)((h * 128 + gw * 32 + r) * DM + b * 128 + c * 8) * 2u, tb);
      tile_store(lds + L_T + TILE, gw, r, c, ta);
      lds_arrive(cnt + C_T1, lane);
    }
  }
  u32x4 bf[8], af[4];
  unsigned raw[32], gr[16];
  f32x4 d0 = {0.f, 0.f, 0.f, 0.f}, d1 = {0.f, 0.f, 0.f, 0.f};
  lds_wait<1>(cnt + C_XN, NHW);
  const unsigned tag = cnt[C_TAG];
  const unsigned pbase = (unsigned)WS_UG + (unsigned)g * (64 * 1024);
  if (!grpA) {
    lds_wait<2>(cnt + C_T1, 4);
    tile_frags(lds + L_T + TILE, gw, r, c, raw);
    pair_tile(raw, bf);
    lds_wait<2>(cnt + C_Y, NHW);
    act_frags(lds + L_Y, r, c, af);
    mfma_tile(af, bf, d0, d1);
    if (mlp) put_rows(rs, pbase + (unsigned)(b * 128 + gw * 32 + c * 2) * 8u, 8 * 1024, tag, r, d0, d1);
  }
  tile_store(lds + L_T + (grpA ? 2 : 3) * TILE, gw, r, c, tb);
  lds_arrive(cnt + (grpA ? C_T2 : C_T3), lane);
  if (hw == 4 && mlp) {
    const unsigned b1p = *reinterpret_cast<const unsigned*>(p.b1 + g * 128 + lane * 2);
    __builtin_amdgcn_s_sleep(64);
    sweep8<64, true>(rs, pbase + (unsigned)b * (8 * 1024) + (unsigned)lane * 16u, 2048, 1024, tag, 0xffu, gr);
    float t0 = 0.f, t1 = 0.f;
#pragma unroll
    for (int j = 0; j < 8; ++j) { t0 += __uint_as_float(gr[2 * j]); t1 += __uint_as_float(gr[2 * j + 1]); }
    t0 = bfr(bfr(t0) + bf_lo(b1p)); t1 = bfr(bfr(t1) + bf_hi(b1p));
    const float e0 = 0.5f * t0 * (1.f + tanhf(0.7978845608028654f * (t0 + 0.044715f * t0 * t0 * t0)));
    const float e1 = 0.5f * t1 * (1.f + tanhf(0.7978845608028654f * (t1 + 0.044715f * t1 * t1 * t1)));
    put_granule((gu64*)(p.ws + WS_HG) + (size_t)(g * NB + b) * 64 + lane, tag, pack_bf16(e0, e1));
    STAMP(4);
  }
  lds_wait<2>(cnt + (grpA ? C_T2 : C_T3), 4);
  tile_frags(lds + L_T + (grpA ? 2 : 3) * TILE, gw, r, c, raw);
  pair_tile(raw, bf);
#pragma unroll
  for (int k = 0; k < 8; ++k) asm volatile("" : "+v"(bf[k]));
  d0 = f32x4{0.f, 0.f, 0.f, 0.f}; d1 = f32x4{0.f, 0.f, 0.f, 0.f};
  f32x4* dx = reinterpret_cast<f32x4*>(lds + L_D + (gw * 64 + lane) * 8);
  if (grpA) {
    if (mlp) {
      if (hw == 0) {
        __builtin_amdgcn_s_sleep(127); __builtin_amdgcn_s_sleep(127);
        sweep8<127, true>(rs, (unsigned)WS_HG + (unsigned)g * 4096u + (unsigned)lane * 16u, 2048, 1024, tag, 0x0fu, gr);
#pragma unroll
        for (int j = 0; j < 4; ++j) *reinterpret_cast<u32x2*>(lds + L_H + j * 128 + lane * 2) = u32x2{gr[2 * j], gr[2 * j + 1]};
        lds_arrive(cnt + C_H, lane);
      }
      lds_wait<32>(cnt + C_H, 1);
      act_frags(lds + L_H, r, c, af);
      mfma_tile(af, bf, d0, d1);
    }
    dx[0] = d0; dx[1] = d1;
    lds_arrive(cnt + C_D, lane);
    STAMP(32 + hw);
    if (hw == 0) {
      const int ob = bid & 7, on = (bid >> 3) * 32 + c * 2;
      const unsigned xv = *reinterpret_cast<const unsigned*>(p.x + ob * DM + on);
      const unsigned bv = *reinterpret_cast<const unsigned*>(p.b2 + on);
      lds_wait<4>(cnt + C_O, 1);
      sweep8<PSLEEP, true>(rs, (unsigned)(WS_PG + ((size_t)((ob * 32 + (bid >> 3)) * NH + r * 8) * 32 + c * 2) * 8), 512, 256, tag, 0xffu, gr);
      float t0 = 0.f, t1 = 0.f;
#pragma unroll
      for (int j = 0; j < 8; ++j) { t0 += __uint_as_float(gr[2 * j]); t1 += __uint_as_float(gr[2 * j + 1]); }
      t0 = xrow_sum(t0) + (bf_lo(xv) + bf_lo(bv));
      t1 = xrow_sum(t1) + (bf_hi(xv) + bf_hi(bv));
      if (r == 0) *reinterpret_cast<unsigned*>(p.out + ob * DM + on) = pack_bf16(t0, t1);
      if (bid == 0 && lane == 0) __hip_atomic_store((gu32*)(p.ws + WS_G), tag, RLX_AGENT);
      STAMP(5);
    }
  } else {
    lds_wait<32>(cnt + C_D, 4);
    d0 = dx[0]; d1 = dx[1];
    if (hw == 4) {
      lds_wait<4>(cnt + C_STREAM, NSW);
      sweep8<PSLEEP, true>(rs, (unsigned)WS_OG + (unsigned)h * 4096u + (unsigned)lane * 16u, 2048, 1024, tag, 0x0fu, gr);
#pragma unroll
      for (int j = 0; j < 4; ++j) *reinterpret_cast<u32x2*>(lds + L_H + j * 128 + lane * 2) = u32x2{gr[2 * j], gr[2 * j + 1]};
      lds_arrive(cnt + C_O, lane);
    } else {
      lds_wait<8>(cnt + C_STREAM, NSW);
    }
    lds_wait<1>(cnt + C_O, 1);
    act_frags(lds + L_H, r, c, af);
    mfma_tile(af, bf, d0, d1);
    put_rows(rs, (unsigned)WS_PG + (unsigned)(((b * 4 + gw) * NH + h) * 32 + c * 2) * 8u, 32 * NH * 32 * 8, tag, r, d0, d1);
    STAMP(40 + gw);
  }
}

}

extern "C" void kernel_launch(void* const* d_in, const int* in_sizes, int n_in, void* d_out, int out_size, void* d_ws,
                              size_t ws_size, hipStream_t stream) {
  Params p;
  p.x = (const unsigned short*)d_in[0];
  p.kc = (const unsigned short*)d_in[1];
  p.vc = (const unsigned short*)d_in[2];
  p.wq = (const unsigned short*)d_in[3];
  p.wo = (const unsigned short*)d_in[4];
  p.w1 = (const unsigned short*)d_in[6];
  p.b1 = (const unsigned short*)d_in[7];
  p.w2 = (const unsigned short*)d_in[8];
  p.b2 = (const unsigned short*)d_in[9];
  p.out = (unsigned short*)d_out;
  p.ws = (unsigned char*)d_ws;
  fused_decode_kernel<<<dim3(NB * NH), dim3(NT), 0, stream>>>(p);
}
```

```cpp
#include <hip/hip_runtime.h>
#include <hip/hip_bf16.h>
#include <stdint.h>

#define MIMIC 1
#define USE_MEMSET 0

namespace {

constexpr int NB = 8, DM = 1024, NH = 32, SK = 4096, DH = 128, FF = 3584, HD = NH * DH;
#define CFG_NSW 8
#define CFG_NL 2
#define CFG_XB 3
#define CFG_DELAY 0
#define CFG_ROT 0
#define PACE_END 0
#define PACE_BEG 400
#define CFG_WIN 1
#define TWIN 2
#define PSLEEP 4
#define KAUX 2
#define VAUX 2
#define WAUX 2
constexpr int NSW = CFG_NSW, NHW = 8, NT = (NSW + NHW) * 64;
constexpr int NL = CFG_NL;
constexpr int CHK = NL * 4;
constexpr int NCH = SK / CHK / NSW;
constexpr unsigned SPIN_MAX = 200000u;

constexpr int NG = FF / 128;
constexpr size_t WS_G = 0;
constexpr size_t WS_OG = 4096;
constexpr size_t WS_HG = WS_OG + (size_t)NH * NB * 64 * 8;
constexpr size_t WS_QG = WS_HG + (size_t)NG * NB * 64 * 8;
constexpr size_t WS_UG = WS_QG + (size_t)NH * NB * NB * 128 * 8;
constexpr size_t WS_PG = WS_UG + (size_t)NG * NB * NB * 128 * 8;
constexpr size_t WS_END = WS_PG + (size_t)NB * DM * NH * 8;

typedef __attribute__((ext_vector_type(2))) __bf16 bf16x2;
typedef __attribute__((ext_vector_type(2))) float f32x2;
typedef __attribute__((ext_vector_type(4))) float f32x4;
typedef __attribute__((ext_vector_type(4))) unsigned u32x4;
typedef __attribute__((ext_vector_type(2))) unsigned u32x2;
typedef __attribute__((ext_vector_type(8))) __bf16 bf16x8;
typedef __attribute__((address_space(1))) unsigned gu32;
typedef __attribute__((address_space(1))) unsigned long long gu64;
#define RLX_AGENT __ATOMIC_RELAXED, __HIP_MEMORY_SCOPE_AGENT

__device__ __forceinline__ float dot2(unsigned a, unsigned b, float acc) {
  return __builtin_amdgcn_fdot2_f32_bf16(__builtin_bit_cast(bf16x2, a), __builtin_bit_cast(bf16x2, b), acc, false);
}
__device__ __forceinline__ unsigned pack_bf16(float lo, float hi) {
  f32x2 v = {lo, hi};
  return __builtin_bit_cast(unsigned, __builtin_convertvector(v, bf16x2));
}
__device__ __forceinline__ float bf_lo(unsigned u) { return __builtin_bit_cast(float, u << 16); }
__device__ __forceinline__ float bf_hi(unsigned u) { return __builtin_bit_cast(float, u & 0xffff0000u); }
__device__ __forceinline__ float bfr(float x) { return bf_lo(pack_bf16(x, 0.f)); }
template <int CTRL> __device__ __forceinline__ float dpp(float x) {
  return __builtin_bit_cast(float, __builtin_amdgcn_mov_dpp(__builtin_bit_cast(int, x), CTRL, 0xf, 0xf, true));
}
constexpr int XOR1 = 0xB1, XOR2 = 0x4E, XOR7 = 0x141, XOR8 = 0x128;
__device__ __forceinline__ float row_sum(float x) {
  x += dpp<XOR1>(x); x += dpp<XOR2>(x); x += dpp<XOR7>(x); x += dpp<XOR8>(x); return x;
}
__device__ __forceinline__ float xrow_sum(float x) {
  auto s = __builtin_amdgcn_permlane16_swap(__float_as_uint(x), __float_as_uint(x), false, false);
  x = __uint_as_float(s[0]) + __uint_as_float(s[1]);
  auto t = __builtin_amdgcn_permlane32_swap(__float_as_uint(x), __float_as_uint(x), false, false);
  return __uint_as_float(t[0]) + __uint_as_float(t[1]);
}
__device__ __forceinline__ float xrow_max(float x) {
  auto s = __builtin_amdgcn_permlane16_swap(__float_as_uint(x), __float_as_uint(x), false, false);
  x = fmaxf(__uint_as_float(s[0]), __uint_as_float(s[1]));
  auto t = __builtin_amdgcn_permlane32_swap(__float_as_uint(x), __float_as_uint(x), false, false);
  return fmaxf(__uint_as_float(t[0]), __uint_as_float(t[1]));
}
__device__ __forceinline__ float wave_sum(float x) { return xrow_sum(row_sum(x)); }
__device__ __forceinline__ unsigned perm_lo(unsigned hi_src, unsigned lo_src) { return __builtin_amdgcn_perm(hi_src, lo_src, 0x05040100u); }
__device__ __forceinline__ unsigned perm_hi(unsigned hi_src, unsigned lo_src) { return __builtin_amdgcn_perm(hi_src, lo_src, 0x07060302u); }
__device__ __forceinline__ u32x4 ld16(const void* p) { return *reinterpret_cast<const u32x4*>(p); }
__device__ __forceinline__ u32x4 ld16nt(const void* p) { return __builtin_nontemporal_load(reinterpret_cast<const u32x4*>(p)); }

template <int AUX>
__device__ __forceinline__ u32x4 ldkv(__amdgpu_buffer_rsrc_t rs, unsigned vo, unsigned so, int i) {
  return __builtin_amdgcn_raw_buffer_load_b128(rs, vo + (i & 3) * 1024, so + (i >> 2) * 4096, AUX);
}

struct LnStat { float sub, den; };
__device__ __forceinline__ LnStat ln_stat(const float (&v)[16]) {
  float s = 0.f;
#pragma unroll
  for (int i = 0; i < 16; ++i) s += v[i];
  const float mean = wave_sum(s) * (1.f / DM);
  float q = 0.f;
#pragma unroll
  for (int i = 0; i < 16; ++i) { const float d = v[i] - mean; q = fmaf(d, d, q); }
  const float var = wave_sum(q) * (1.f / DM);
  LnStat st;
  st.sub = bfr(mean);
  st.den = bfr(sqrtf(bfr(bfr(var) + 1e-5f)));
  return st;
}
__device__ __forceinline__ float ln_apply(float x, LnStat st) {
  return bfr(x - st.sub) / st.den;
}
__device__ __forceinline__ void unpack16(u32x4 a, u32x4 b, float (&v)[16]) {
#pragma unroll
  for (int j = 0; j < 4; ++j) { v[2 * j] = bf_lo(a[j]); v[2 * j + 1] = bf_hi(a[j]); v[8 + 2 * j] = bf_lo(b[j]); v[8 + 2 * j + 1] = bf_hi(b[j]); }
}
#define STAMP(i) do {} while (0)

struct Params {
  const unsigned short* x; const unsigned short* kc; const unsigned short* vc; const unsigned short* wq; const unsigned short* wo;
  const unsigned short* w1; const unsigned short* b1; const unsigned short* w2; const unsigned short* b2;
  unsigned short* out; unsigned char* ws;
};

constexpr int TROW = 68;
constexpr int TILE = 128 * TROW;
constexpr int L_T = 0;
constexpr int L_Q = L_T + 4 * TILE;
constexpr int L_XN = L_Q + 64;
constexpr int L_Y = L_XN + 512;
constexpr int L_H = L_Y + 512;
constexpr int L_D = L_H + 512;
constexpr int L_REC = L_D + 2048;
constexpr int REC = 132;
constexpr int L_CNT = L_REC + NSW * REC;
constexpr int L_END = L_CNT + 16;
enum { C_STREAM = 0, C_XN, C_Y, C_TAG, C_Q, C_T0, C_T1, C_T2, C_T3, C_H, C_D, C_ROT, C_O };
constexpr unsigned OOB = 0xfffffff0u;

template <int N> __device__ __forceinline__ void window() {
  __builtin_amdgcn_sched_barrier(0);
  asm volatile("s_waitcnt vmcnt(%0)" ::"i"(N) : "memory");
  __builtin_amdgcn_sched_barrier(0);
}
template <int ROWB, bool WIN>
__device__ __forceinline__ void tile_load(__amdgpu_buffer_rsrc_t rw, unsigned off, u32x4 (&t)[8]) {
#pragma unroll
  for (int i = 0; i < 8; ++i) {
    if (WIN && (i & 1) == 0) window<TWIN>();
    t[i] = __builtin_amdgcn_raw_buffer_load_b128(rw, off, i * 4 * ROWB, WAUX);
  }
}
__device__ __forceinline__ void tile_store(float* tile, int gw, int r, int c, const u32x4 (&t)[8]) {
#pragma unroll
  for (int i = 0; i < 8; ++i) *reinterpret_cast<u32x4*>(tile + (gw * 32 + i * 4 + r) * TROW + c * 4) = t[i];
}
__device__ __forceinline__ void tile_frags(const float* tile, int gw, int r, int c, unsigned (&raw)[32]) {
#pragma unroll
  for (int i = 0; i < 32; ++i) raw[i] = __float_as_uint(tile[((i >> 3) * 32 + r * 8 + (i & 7)) * TROW + gw * 16 + c]);
}
__device__ __forceinline__ void act_frags(const float* act, int r, int c, u32x4 (&af)[4]) {
#pragma unroll
  for (int kk = 0; kk < 4; ++kk) {
    af[kk] = *reinterpret_cast<const u32x4*>(act + (c & 7) * 64 + kk * 16 + r * 4);
    if (c >= 8) af[kk] = u32x4{0u, 0u, 0u, 0u};
  }
}
__device__ __forceinline__ void pair_tile(const unsigned (&raw)[32], u32x4 (&bf)[8]) {
#pragma unroll
  for (int kk = 0; kk < 4; ++kk)
#pragma unroll
    for (int e = 0; e < 4; ++e) {
      bf[kk * 2][e] = perm_lo(raw[kk * 8 + 2 * e + 1], raw[kk * 8 + 2 * e]);
      bf[kk * 2 + 1][e] = perm_hi(raw[kk * 8 + 2 * e + 1], raw[kk * 8 + 2 * e]);
    }
}
__device__ __forceinline__ void mfma_tile(const u32x4 (&af)[4], const u32x4 (&bf)[8], f32x4& d0, f32x4& d1) {
#pragma unroll
  for (int kk = 0; kk < 4; ++kk) {
    d0 = __builtin_amdgcn_mfma_f32_16x16x32_bf16(__builtin_bit_cast(bf16x8, af[kk]), __builtin_bit_cast(bf16x8, bf[kk * 2]), d0, 0, 0, 0);
    d1 = __builtin_amdgcn_mfma_f32_16x16x32_bf16(__builtin_bit_cast(bf16x8, af[kk]), __builtin_bit_cast(bf16x8, bf[kk * 2 + 1]), d1, 0, 0, 0);
  }
}
template <int SLEEP, bool NARROW>
__device__ __forceinline__ void sweep8(__amdgpu_buffer_rsrc_t rs, unsigned off0, unsigned shi, unsigned slo, unsigned tag,
                                       unsigned pend, unsigned (&v)[16]) {
#pragma unroll
  for (int j = 0; j < 16; ++j) v[j] = 0u;
  unsigned act = pend;
  for (unsigned spins = 0; spins < SPIN_MAX; ++spins) {
    u32x4 g[8];
#pragma unroll
    for (int j = 0; j < 8; ++j) {
      const unsigned off = ((act >> j) & 1u) ? off0 + (j >> 1) * shi + (j & 1) * slo : OOB;
      g[j] = __builtin_amdgcn_raw_buffer_load_b128(rs, off, 0, 16);
    }
    __builtin_amdgcn_sched_barrier(0);
    const unsigned before = pend;
#pragma unroll
    for (int j = 0; j < 8; ++j) {
      const bool ok = ((act >> j) & 1u) && g[j][1] == tag && g[j][3] == tag;
      v[2 * j] = ok ? g[j][0] : v[2 * j];
      v[2 * j + 1] = ok ? g[j][2] : v[2 * j + 1];
      pend = ok ? (pend & ~(1u << j)) : pend;
    }
    if (!__any(pend != 0u)) break;
    if (NARROW) {
      const bool was_narrow_hit = __any(pend != before && act != before);
      act = was_narrow_hit ? pend : (pend & (0u - pend));
      if (!was_narrow_hit) __builtin_amdgcn_s_sleep(SLEEP);
    } else {
      act = pend;
      __builtin_amdgcn_s_sleep(SLEEP);
    }
  }
}
__device__ __forceinline__ void put_granule(gu64* g, unsigned tag, unsigned val) {
  __hip_atomic_store(g, ((unsigned long long)tag << 32) | val, RLX_AGENT);
}
__device__ __forceinline__ void put_rows(__amdgpu_buffer_rsrc_t rs, unsigned off, unsigned rstride, unsigned tag, int r,
                                         const f32x4& d0, const f32x4& d1) {
  if (r < 2) {
#pragma unroll
    for (int i = 0; i < 4; ++i)
      __builtin_amdgcn_raw_buffer_store_b128(u32x4{__float_as_uint(d0[i]), tag, __float_as_uint(d1[i]), tag}, rs,
                                             off + (r * 4 + i) * rstride, 0, 16);
  }
}
__device__ __forceinline__ void lds_arrive(unsigned* cnt, int lane) {
  asm volatile("s_waitcnt lgkmcnt(0)" ::: "memory");
  if (lane == 0) __hip_atomic_fetch_add(cnt, 1u, __ATOMIC_RELAXED, __HIP_MEMORY_SCOPE_WORKGROUP);
}
template <int SLEEP>
__device__ __forceinline__ void lds_wait(unsigned* cnt, unsigned n) {
  for (unsigned spins = 0; spins < 50u * SPIN_MAX; ++spins) {
    if (__hip_atomic_load(cnt, __ATOMIC_RELAXED, __HIP_MEMORY_SCOPE_WORKGROUP) >= n) break;
    __builtin_amdgcn_s_sleep(SLEEP);
  }
  asm volatile("" ::: "memory");
}

template <bool PF>
__device__ __forceinline__ void attn_step(u32x4 (&Kb)[NL], u32x4 (&Vb)[NL], __amdgpu_buffer_rsrc_t rsk, __amdgpu_buffer_rsrc_t rsv, unsigned so, unsigned vo,
                                          const u32x4& q, float& m, float& l, float (&acc)[8]) {
  float s[NL];
#pragma unroll
  for (int i = 0; i < NL; ++i) {
    float d = dot2(Kb[i][0], q[0], 0.f);
    d = dot2(Kb[i][1], q[1], d); d = dot2(Kb[i][2], q[2], d); d = dot2(Kb[i][3], q[3], d);
    s[i] = d;
  }
  if (PF) {
#pragma unroll
    for (int i = 0; i < NL; ++i) Kb[i] = ldkv<KAUX>(rsk, vo, so, i);
  }
  __builtin_amdgcn_sched_barrier(0);
  constexpr float SCALE = 0.08838834764831845f, LOG2E = 1.4426950408889634f;
#pragma unroll
  for (int i = 0; i < NL; ++i) s[i] = row_sum(s[i]);
#pragma unroll
  for (int i = 0; i < NL; i += 2) {
    unsigned u = pack_bf16(s[i], s[i + 1]);
    u = pack_bf16(bf_lo(u) * SCALE, bf_hi(u) * SCALE);
    s[i] = bf_lo(u) * LOG2E; s[i + 1] = bf_hi(u) * LOG2E;
  }
  float mn = m;
#pragma unroll
  for (int i = 0; i < NL; ++i) mn = fmaxf(mn, s[i]);
  const float alpha = __builtin_amdgcn_exp2f(m - mn);
  m = mn;
  float ps = 0.f;
#pragma unroll
  for (int i = 0; i < NL; ++i) { s[i] = __builtin_amdgcn_exp2f(s[i] - mn); ps += s[i]; }
  l = fmaf(l, alpha, ps);
#pragma unroll
  for (int k = 0; k < 8; ++k) acc[k] *= alpha;
#pragma unroll
  for (int i = 0; i < NL; ++i) {
#pragma unroll
    for (int j = 0; j < 4; ++j) {
      acc[2 * j] = fmaf(s[i], bf_lo(Vb[i][j]), acc[2 * j]);
      acc[2 * j + 1] = fmaf(s[i], bf_hi(Vb[i][j]), acc[2 * j + 1]);
    }
  }
  if (PF) {
#pragma unroll
    for (int i = 0; i < NL; ++i) Vb[i] = ldkv<VAUX>(rsv, vo, so, i);
  }
  __builtin_amdgcn_sched_barrier(0);
}

__device__ __forceinline__ void q_phase(const Params& p, float* lds, __amdgpu_buffer_rsrc_t rs, int h, int b, int hw, int lane) {
  unsigned* cnt = reinterpret_cast<unsigned*>(lds) + L_CNT;
  const int r = lane >> 4, c = lane & 15;
  u32x4 bf[8], af[4];
  unsigned raw[32];
  f32x4 d0 = {0.f, 0.f, 0.f, 0.f}, d1 = {0.f, 0.f, 0.f, 0.f};
  lds_wait<1>(cnt + C_T0, 4);
  tile_frags(lds + L_T, hw, r, c, raw);
  pair_tile(raw, bf);
  lds_wait<1>(cnt + C_XN, NHW);
  const unsigned tag = cnt[C_TAG];
  act_frags(lds + L_XN, r, c, af);
  mfma_tile(af, bf, d0, d1);
  const unsigned pbase = (unsigned)WS_QG + (unsigned)h * (64 * 1024);
  put_rows(rs, pbase + (unsigned)(b * 128 + hw * 32 + c * 2) * 8u, 8 * 1024, tag, r, d0, d1);
  if (hw == 0) {
    unsigned gr[16];
    sweep8<1, false>(rs, pbase + (unsigned)b * (8 * 1024) + (unsigned)lane * 16u, 2048, 1024, tag, 0xffu, gr);
    float t0 = 0.f, t1 = 0.f;
#pragma unroll
    for (int j = 0; j < 8; ++j) { t0 += __uint_as_float(gr[2 * j]); t1 += __uint_as_float(gr[2 * j + 1]); }
    reinterpret_cast<unsigned*>(lds)[L_Q + lane] = pack_bf16(t0, t1);
    lds_arrive(cnt + C_Q, lane);
  }
}

__global__ void __launch_bounds__(NT, NT / 256) fused_decode_kernel(Params p) {
  __shared__ __attribute__((aligned(16))) float lds[L_END];
  unsigned* ldsu = reinterpret_cast<unsigned*>(lds);
  const int bid = blockIdx.x, xcd = bid & 7, idx = bid >> 3;
  const int h = xcd * 4 + (idx >> 3), b = idx & 7;
  const int tid = threadIdx.x, lane = tid & 63, wave = __builtin_amdgcn_readfirstlane(tid >> 6);
  const int r = lane >> 4, c = lane & 15;
  const bool streamer = wave >= NHW;
  if (wave == 0) STAMP(0);

  if (streamer) {
    const int sw = wave - NHW;
    const size_t slab = (size_t)(b * NH + h) * SK * DH;
    const __amdgpu_buffer_rsrc_t rsk = __builtin_amdgcn_make_buffer_rsrc((void*)(p.kc + slab), 0, SK * DH * 2, 0x00020000);
    const __amdgpu_buffer_rsrc_t rsv = __builtin_amdgcn_make_buffer_rsrc((void*)(p.vc + slab), 0, SK * DH * 2, 0x00020000);
    unsigned so = (unsigned)sw * (CHK * DH * 2);
    const unsigned vo = (unsigned)lane * 16u;
    constexpr int STRIDE = NSW * CHK * DH * 2;
    constexpr int XB = CFG_XB;
    u32x4 Kb[NL], Vb[NL], Kx[XB > 0 ? XB : 1][NL], Vx[XB > 0 ? XB : 1][NL];
    __syncthreads();
    if (CFG_DELAY) __builtin_amdgcn_s_sleep(CFG_DELAY);
#pragma unroll
    for (int i = 0; i < NL; ++i) Kb[i] = ldkv<KAUX>(rsk, vo, so, i);
#pragma unroll
    for (int i = 0; i < NL; ++i) Vb[i] = ldkv<VAUX>(rsv, vo, so, i);
#pragma unroll
    for (int x = 0; x < XB; ++x) {
      window<NL>();
#pragma unroll
      for (int i = 0; i < NL; ++i) Kx[x][i] = ldkv<KAUX>(rsk, vo, so + (NCH - 1 - x) * STRIDE, i);
      window<NL>();
#pragma unroll
      for (int i = 0; i < NL; ++i) Vx[x][i] = ldkv<VAUX>(rsv, vo, so + (NCH - 1 - x) * STRIDE, i);
    }
    __builtin_amdgcn_sched_barrier(0);
    lds_wait<2>(ldsu + L_CNT + C_Q, 1);
    if (sw == 0) STAMP(2);
    const unsigned tag = ldsu[L_CNT + C_TAG];
    const u32x4 q = *reinterpret_cast<const u32x4*>(lds + L_Q + c * 4);
    float m = -INFINITY, l = 0.f, acc[8];
#pragma unroll
    for (int k = 0; k < 8; ++k) acc[k] = 0.f;
    if (XB > 0) {
      so += STRIDE;
      attn_step<true>(Kb, Vb, rsk, rsv, so, vo, q, m, l, acc);
#pragma unroll
      for (int x = 0; x < XB; ++x) attn_step<false>(Kx[x], Vx[x], rsk, rsv, so, vo, q, m, l, acc);
    }
    for (int it = (XB > 0 ? 1 : 0); it < NCH - 1 - XB; ++it) {
      so += STRIDE;
      attn_step<true>(Kb, Vb, rsk, rsv, so, vo, q, m, l, acc);
    }
    attn_step<false>(Kb, Vb, rsk, rsv, so, vo, q, m, l, acc);
    STAMP(16 + sw);
    const float M = xrow_max(m);
    const float wt = __builtin_amdgcn_exp2f(m - M);
    l = xrow_sum(l * wt);
#pragma unroll
    for (int k = 0; k < 8; ++k) acc[k] = xrow_sum(acc[k] * wt);
    float* rec = lds + L_REC + sw * REC;
    if (r == 0) {
      f32x4* d = reinterpret_cast<f32x4*>(rec + 4 + c * 8);
      d[0] = f32x4{acc[0], acc[1], acc[2], acc[3]}; d[1] = f32x4{acc[4], acc[5], acc[6], acc[7]};
      if (c == 0) { rec[0] = M; rec[1] = l; }
    }
    unsigned arrived = 0;
    if (lane == 0) arrived = __hip_atomic_fetch_add(ldsu + L_CNT + C_STREAM, 1u, __ATOMIC_ACQ_REL, __HIP_MEMORY_SCOPE_WORKGROUP);
    arrived = __builtin_amdgcn_readfirstlane(arrived);
    if (arrived == NSW - 1) {
      __builtin_amdgcn_fence(__ATOMIC_ACQUIRE, "workgroup");
      float MM = -INFINITY;
#pragma unroll
      for (int w = 0; w < NSW; ++w) MM = fmaxf(MM, lds[L_REC + w * REC]);
      float L = 0.f, o0 = 0.f, o1 = 0.f;
#pragma unroll
      for (int w = 0; w < NSW; ++w) {
        const float* rw = lds + L_REC + w * REC;
        const float e = __builtin_amdgcn_exp2f(rw[0] - MM);
        const f32x2 a = *reinterpret_cast<const f32x2*>(rw + 4 + 2 * lane);
        L = fmaf(e, rw[1], L); o0 = fmaf(e, a[0], o0); o1 = fmaf(e, a[1], o1);
      }
      const float inv = 1.f / L;
      const unsigned pay = pack_bf16(o0 * inv, o1 * inv);
      gu64* og = (gu64*)(p.ws + WS_OG) + (size_t)(h * NB + b) * 64 + lane;
      __hip_atomic_store(og, ((unsigned long long)tag << 32) | pay, RLX_AGENT);
      STAMP(3);
    }
    return;
  }

  const int hw = wave, gw = wave & 3;
  const bool grpA = hw < 4;
  __builtin_amdgcn_s_setprio(3);
  unsigned* cnt = ldsu + L_CNT;
  const __amdgpu_buffer_rsrc_t rs = __builtin_amdgcn_make_buffer_rsrc(p.ws, 0, (int)WS_END, 0x00020000);
  const bool mlp = idx < 24 || ((idx < 28) == ((xcd & 1) == 0));
  const int g = idx < 24 ? xcd * 3 + (idx >> 3) : 24 + (xcd >> 1);

  const unsigned short* xr = p.x + hw * DM + lane * 16;
  const u32x4 x0 = ld16(xr), x1 = ld16(xr + 8);
  unsigned g0 = 0;
  g0 = __hip_atomic_load((gu32*)(p.ws + WS_G), RLX_AGENT);
  u32x4 ta[8], tb[8];
  if (grpA) {
    const __amdgpu_buffer_rsrc_t r0 = __builtin_amdgcn_make_buffer_rsrc((void*)p.wq, 0, DM * HD * 2, 0x00020000);
    tile_load<HD * 2, false>(r0, (unsigned)((b * 128 + gw * 32 + r) * HD + h * 128 + c * 8) * 2u, ta);
  }
  if (tid < 16) cnt[tid] = 0u;
  __builtin_amdgcn_sched_barrier(0);
  __syncthreads();
  {
    float v[16];
    unpack16(x0, x1, v);
    const LnStat s1 = ln_stat(v);
    u32x4 y0, y1;
#pragma unroll
    for (int j = 0; j < 4; ++j) {
      y0[j] = pack_bf16(ln_apply(v[2 * j], s1), ln_apply(v[2 * j + 1], s1));
      y1[j] = pack_bf16(ln_apply(v[8 + 2 * j], s1), ln_apply(v[8 + 2 * j + 1], s1));
    }
    if ((lane >> 3) == b) {
      u32x4* xd = reinterpret_cast<u32x4*>(lds + L_XN + hw * 64 + (lane & 7) * 8);
      xd[0] = y0; xd[1] = y1;
    }
    if (tid == 0) cnt[C_TAG] = g0 + 1u;
    lds_arrive(cnt + C_XN, lane);
    if (grpA) {
      tile_store(lds + L_T, gw, r, c, ta);
      lds_arrive(cnt + C_T0, lane);
      q_phase(p, lds, rs, h, b, hw, lane);
      const __amdgpu_buffer_rsrc_t r1 = __builtin_amdgcn_make_buffer_rsrc((void*)p.w2, 0, mlp ? FF * DM * 2 : 0, 0x00020000);
      tile_load<DM * 2, true>(r1, (unsigned)((g * 128 + gw * 32 + r) * DM + b * 128 + c * 8) * 2u, tb);
    }
    __builtin_amdgcn_sched_barrier(0);
    unpack16(y0, y1, v);
    const LnStat s2 = ln_stat(v);
#pragma unroll
    for (int j = 0; j < 4; ++j) {
      y0[j] = pack_bf16(ln_apply(v[2 * j], s2), ln_apply(v[2 * j + 1], s2));
      y1[j] = pack_bf16(ln_apply(v[8 + 2 * j], s2), ln_apply(v[8 + 2 * j + 1], s2));
    }
    if ((lane >> 3) == b) {
      u32x4* yd = reinterpret_cast<u32x4*>(lds + L_Y + hw * 64 + (lane & 7) * 8);
      yd[0] = y0; yd[1] = y1;
    }
    lds_arrive(cnt + C_Y, lane);
    if (!grpA) {
      const __amdgpu_buffer_rsrc_t r0 = __builtin_amdgcn_make_buffer_rsrc((void*)p.w1, 0, mlp ? DM * FF * 2 : 0, 0x00020000);
      tile_load<FF * 2, true>(r0, (unsigned)((b * 128 + gw * 32 + r) * FF + g * 128 + c * 8) * 2u, ta);
      const __amdgpu_buffer_rsrc_t r1 = __builtin_amdgcn_make_buffer_rsrc((void*)p.wo, 0, HD * DM * 2, 0x00020000);
      tile_load<DM * 2, true>(r1, (unsigned)((h * 128 + gw * 32 + r) * DM + b * 128 + c * 8) * 2u, tb);
      tile_store(lds + L_T + TILE, gw, r, c, ta);
      lds_arrive(cnt + C_T1, lane);
    }
  }
  u32x4 bf[8], af[4];
  unsigned raw[32], gr[16];
  f32x4 d0 = {0.f, 0.f, 0.f, 0.f}, d1 = {0.f, 0.f, 0.f, 0.f};
  lds_wait<1>(cnt + C_XN, NHW);
  const unsigned tag = cnt[C_TAG];
  const unsigned pbase = (unsigned)WS_UG + (unsigned)g * (64 * 1024);
  if (!grpA) {
    lds_wait<2>(cnt + C_T1, 4);
    tile_frags(lds + L_T + TILE, gw, r, c, raw);
    pair_tile(raw, bf);
    lds_wait<2>(cnt + C_Y, NHW);
    act_frags(lds + L_Y, r, c, af);
    mfma_tile(af, bf, d0, d1);
    if (mlp) put_rows(rs, pbase + (unsigned)(b * 128 + gw * 32 + c * 2) * 8u, 8 * 1024, tag, r, d0, d1);
  }
  tile_store(lds + L_T + (grpA ? 2 : 3) * TILE, gw, r, c, tb);
  lds_arrive(cnt + (grpA ? C_T2 : C_T3), lane);
  if (hw == 4 && mlp) {
    const unsigned b1p = *reinterpret_cast<const unsigned*>(p.b1 + g * 128 + lane * 2);
    __builtin_amdgcn_s_sleep(64);
    sweep8<64, true>(rs, pbase + (unsigned)b * (8 * 1024) + (unsigned)lane * 16u, 2048, 1024, tag, 0xffu, gr);
    float t0 = 0.f, t1 = 0.f;
#pragma unroll
    for (int j = 0; j < 8; ++j) { t0 += __uint_as_float(gr[2 * j]); t1 += __uint_as_float(gr[2 * j + 1]); }
    t0 = bfr(bfr(t0) + bf_lo(b1p)); t1 = bfr(bfr(t1) + bf_hi(b1p));
    const float e0 = 0.5f * t0 * (1.f + tanhf(0.7978845608028654f * (t0 + 0.044715f * t0 * t0 * t0)));
    const float e1 = 0.5f * t1 * (1.f + tanhf(0.7978845608028654f * (t1 + 0.044715f * t1 * t1 * t1)));
    put_granule((gu64*)(p.ws + WS_HG) + (size_t)(g * NB + b) * 64 + lane, tag, pack_bf16(e0, e1));
    STAMP(4);
  }
  lds_wait<2>(cnt + (grpA ? C_T2 : C_T3), 4);
  tile_frags(lds + L_T + (grpA ? 2 : 3) * TILE, gw, r, c, raw);
  pair_tile(raw, bf);
#pragma unroll
  for (int k = 0; k < 8; ++k) asm volatile("" : "+v"(bf[k]));
  d0 = f32x4{0.f, 0.f, 0.f, 0.f}; d1 = f32x4{0.f, 0.f, 0.f, 0.f};
  f32x4* dx = reinterpret_cast<f32x4*>(lds + L_D + (gw * 64 + lane) * 8);
  if (grpA) {
    if (mlp) {
      if (hw == 0) {
        __builtin_amdgcn_s_sleep(127); __builtin_amdgcn_s_sleep(127);
        sweep8<127, true>(rs, (unsigned)WS_HG + (unsigned)g * 4096u + (unsigned)lane * 16u, 2048, 1024, tag, 0x0fu, gr);
#pragma unroll
        for (int j = 0; j < 4; ++j) *reinterpret_cast<u32x2*>(lds + L_H + j * 128 + lane * 2) = u32x2{gr[2 * j], gr[2 * j + 1]};
        lds_arrive(cnt + C_H, lane);
      }
      lds_wait<32>(cnt + C_H, 1);
      act_frags(lds + L_H, r, c, af);
      mfma_tile(af, bf, d0, d1);
    }
    dx[0] = d0; dx[1] = d1;
    lds_arrive(cnt + C_D, lane);
    STAMP(32 + hw);
    if (hw == 0) {
      const int ob = bid & 7, on = (bid >> 3) * 32 + c * 2;
      const unsigned xv = *reinterpret_cast<const unsigned*>(p.x + ob * DM + on);
      const unsigned bv = *reinterpret_cast<const unsigned*>(p.b2 + on);
      lds_wait<4>(cnt + C_O, 1);
      sweep8<PSLEEP, true>(rs, (unsigned)(WS_PG + ((size_t)((ob * 32 + (bid >> 3)) * NH + r * 8) * 32 + c * 2) * 8), 512, 256, tag, 0xffu, gr);
      float t0 = 0.f, t1 = 0.f;
#pragma unroll
      for (int j = 0; j < 8; ++j) { t0 += __uint_as_float(gr[2 * j]); t1 += __uint_as_float(gr[2 * j + 1]); }
      t0 = xrow_sum(t0) + (bf_lo(xv) + bf_lo(bv));
      t1 = xrow_sum(t1) + (bf_hi(xv) + bf_hi(bv));
      if (r == 0) *reinterpret_cast<unsigned*>(p.out + ob * DM + on) = pack_bf16(t0, t1);
      if (bid == 0 && lane == 0) __hip_atomic_store((gu32*)(p.ws + WS_G), tag, RLX_AGENT);
      STAMP(5);
    }
  } else {
    lds_wait<32>(cnt + C_D, 4);
    d0 = dx[0]; d1 = dx[1];
    if (hw == 4) {
      lds_wait<4>(cnt + C_STREAM, NSW);
      sweep8<PSLEEP, true>(rs, (unsigned)WS_OG + (unsigned)h * 4096u + (unsigned)lane * 16u, 2048, 1024, tag, 0x0fu, gr);
#pragma unroll
      for (int j = 0; j < 4; ++j) *reinterpret_cast<u32x2*>(lds + L_H + j * 128 + lane * 2) = u32x2{gr[2 * j], gr[2 * j + 1]};
      lds_arrive(cnt + C_O, lane);
    } else {
      lds_wait<8>(cnt + C_STREAM, NSW);
    }
    lds_wait<1>(cnt + C_O, 1);
    act_frags(lds + L_H, r, c, af);
    mfma_tile(af, bf, d0, d1);
    put_rows(rs, (unsigned)WS_PG + (unsigned)(((b * 4 + gw) * NH + h) * 32 + c * 2) * 8u, 32 * NH * 32 * 8, tag, r, d0, d1);
    STAMP(40 + gw);
  }
}

}

extern "C" void kernel_launch(void* const* d_in, const int* in_sizes, int n_in, void* d_out, int out_size, void* d_ws,
                              size_t ws_size, hipStream_t stream) {
  Params p;
  p.x = (const unsigned short*)d_in[0];
  p.kc = (const unsigned short*)d_in[1];
  p.vc = (const unsigned short*)d_in[2];
  p.wq = (const unsigned short*)d_in[3];
  p.wo = (const unsigned short*)d_in[4];
  p.w1 = (const unsigned short*)d_in[6];
  p.b1 = (const unsigned short*)d_in[7];
  p.w2 = (const unsigned short*)d_in[8];
  p.b2 = (const unsigned short*)d_in[9];
  p.out = (unsigned short*)d_out;
  p.ws = (unsigned char*)d_ws;
  fused_decode_kernel<<<dim3(NB * NH), dim3(NT), 0, stream>>>(p);
}
```

```cpp
#include <hip/hip_runtime.h>
#include <hip/hip_bf16.h>
#include <stdint.h>

#define MIMIC 1
#define USE_MEMSET 0

namespace {

constexpr int NB = 8, DM = 1024, NH = 32, SK = 4096, DH = 128, FF = 3584, HD = NH * DH;
#define CFG_NSW 8
#define CFG_NL 2
#define CFG_XB 3
#define CFG_DELAY 0
#define CFG_ROT 0
#define PACE_END 0
#define PACE_BEG 400
#define DRY 7
#define CFG_WIN 1
#define TWIN 2
#define PSLEEP 4
#define KAUX 2
#define VAUX 2
#define WAUX 2
constexpr int NSW = CFG_NSW, NHW = 8, NT = (NSW + NHW) * 64;
constexpr int NL = CFG_NL;
constexpr int CHK = NL * 4;
constexpr int NCH = SK / CHK / NSW;
constexpr unsigned SPIN_MAX = 200000u;

constexpr int NG = FF / 128;
constexpr size_t WS_G = 0;
constexpr size_t WS_OG = 4096;
constexpr size_t WS_HG = WS_OG + (size_t)NH * NB * 64 * 8;
constexpr size_t WS_QG = WS_HG + (size_t)NG * NB * 64 * 8;
constexpr size_t WS_UG = WS_QG + (size_t)NH * NB * NB * 128 * 8;
constexpr size_t WS_PG = WS_UG + (size_t)NG * NB * NB * 128 * 8;
constexpr size_t WS_END = WS_PG + (size_t)NB * DM * NH * 8;

typedef __attribute__((ext_vector_type(2))) __bf16 bf16x2;
typedef __attribute__((ext_vector_type(2))) float f32x2;
typedef __attribute__((ext_vector_type(4))) float f32x4;
typedef __attribute__((ext_vector_type(4))) unsigned u32x4;
typedef __attribute__((ext_vector_type(2))) unsigned u32x2;
typedef __attribute__((ext_vector_type(8))) __bf16 bf16x8;
typedef __attribute__((address_space(1))) unsigned gu32;
typedef __attribute__((address_space(1))) unsigned long long gu64;
#define RLX_AGENT __ATOMIC_RELAXED, __HIP_MEMORY_SCOPE_AGENT

__device__ __forceinline__ float dot2(unsigned a, unsigned b, float acc) {
  return __builtin_amdgcn_fdot2_f32_bf16(__builtin_bit_cast(bf16x2, a), __builtin_bit_cast(bf16x2, b), acc, false);
}
__device__ __forceinline__ unsigned pack_bf16(float lo, float hi) {
  f32x2 v = {lo, hi};
  return __builtin_bit_cast(unsigned, __builtin_convertvector(v, bf16x2));
}
__device__ __forceinline__ float bf_lo(unsigned u) { return __builtin_bit_cast(float, u << 16); }
__device__ __forceinline__ float bf_hi(unsigned u) { return __builtin_bit_cast(float, u & 0xffff0000u); }
__device__ __forceinline__ float bfr(float x) { return bf_lo(pack_bf16(x, 0.f)); }
template <int CTRL> __device__ __forceinline__ float dpp(float x) {
  return __builtin_bit_cast(float, __builtin_amdgcn_mov_dpp(__builtin_bit_cast(int, x), CTRL, 0xf, 0xf, true));
}
constexpr int XOR1 = 0xB1, XOR2 = 0x4E, XOR7 = 0x141, XOR8 = 0x128;
__device__ __forceinline__ float row_sum(float x) {
  x += dpp<XOR1>(x); x += dpp<XOR2>(x); x += dpp<XOR7>(x); x += dpp<XOR8>(x); return x;
}
__device__ __forceinline__ float xrow_sum(float x) {
  auto s = __builtin_amdgcn_permlane16_swap(__float_as_uint(x), __float_as_uint(x), false, false);
  x = __uint_as_float(s[0]) + __uint_as_float(s[1]);
  auto t = __builtin_amdgcn_permlane32_swap(__float_as_uint(x), __float_as_uint(x), false, false);
  return __uint_as_float(t[0]) + __uint_as_float(t[1]);
}
__device__ __forceinline__ float xrow_max(float x) {
  auto s = __builtin_amdgcn_permlane16_swap(__float_as_uint(x), __float_as_uint(x), false, false);
  x = fmaxf(__uint_as_float(s[0]), __uint_as_float(s[1]));
  auto t = __builtin_amdgcn_permlane32_swap(__float_as_uint(x), __float_as_uint(x), false, false);
  return fmaxf(__uint_as_float(t[0]), __uint_as_float(t[1]));
}
__device__ __forceinline__ float wave_sum(float x) { return xrow_sum(row_sum(x)); }
__device__ __forceinline__ unsigned perm_lo(unsigned hi_src, unsigned lo_src) { return __builtin_amdgcn_perm(hi_src, lo_src, 0x05040100u); }
__device__ __forceinline__ unsigned perm_hi(unsigned hi_src, unsigned lo_src) { return __builtin_amdgcn_perm(hi_src, lo_src, 0x07060302u); }
__device__ __forceinline__ u32x4 ld16(const void* p) { return *reinterpret_cast<const u32x4*>(p); }
__device__ __forceinline__ u32x4 ld16nt(const void* p) { return __builtin_nontemporal_load(reinterpret_cast<const u32x4*>(p)); }

template <int AUX>
__device__ __forceinline__ u32x4 ldkv(__amdgpu_buffer_rsrc_t rs, unsigned vo, unsigned so, int i) {
  return __builtin_amdgcn_raw_buffer_load_b128(rs, vo + (i & 3) * 1024, so + (i >> 2) * 4096, AUX);
}

struct LnStat { float sub, den; };
__device__ __forceinline__ LnStat ln_stat(const float (&v)[16]) {
  float s = 0.f;
#pragma unroll
  for (int i = 0; i < 16; ++i) s += v[i];
  const float mean = wave_sum(s) * (1.f / DM);
  float q = 0.f;
#pragma unroll
  for (int i = 0; i < 16; ++i) { const float d = v[i] - mean; q = fmaf(d, d, q); }
  const float var = wave_sum(q) * (1.f / DM);
  LnStat st;
  st.sub = bfr(mean);
  st.den = bfr(sqrtf(bfr(bfr(var) + 1e-5f)));
  return st;
}
__device__ __forceinline__ float ln_apply(float x, LnStat st) {
  return bfr(x - st.sub) / st.den;
}
__device__ __forceinline__ void unpack16(u32x4 a, u32x4 b, float (&v)[16]) {
#pragma unroll
  for (int j = 0; j < 4; ++j) { v[2 * j] = bf_lo(a[j]); v[2 * j + 1] = bf_hi(a[j]); v[8 + 2 * j] = bf_lo(b[j]); v[8 + 2 * j + 1] = bf_hi(b[j]); }
}
#define STAMP(i) do {} while (0)

struct Params {
  const unsigned short* x; const unsigned short* kc; const unsigned short* vc; const unsigned short* wq; const unsigned short* wo;
  const unsigned short* w1; const unsigned short* b1; const unsigned short* w2; const unsigned short* b2;
  unsigned short* out; unsigned char* ws;
};

constexpr int TROW = 68;
constexpr int TILE = 128 * TROW;
constexpr int L_T = 0;
constexpr int L_Q = L_T + 4 * TILE;
constexpr int L_XN = L_Q + 64;
constexpr int L_Y = L_XN + 512;
constexpr int L_H = L_Y + 512;
constexpr int L_D = L_H + 512;
constexpr int L_REC = L_D + 2048;
constexpr int REC = 132;
constexpr int L_CNT = L_REC + NSW * REC;
constexpr int L_END = L_CNT + 16;
enum { C_STREAM = 0, C_XN, C_Y, C_TAG, C_Q, C_T0, C_T1, C_T2, C_T3, C_H, C_D, C_O, C_DRY0, C_DRY1 };
constexpr unsigned OOB = 0xfffffff0u;

template <int N> __device__ __forceinline__ void window() {
  __builtin_amdgcn_sched_barrier(0);
  asm volatile("s_waitcnt vmcnt(%0)" ::"i"(N) : "memory");
  __builtin_amdgcn_sched_barrier(0);
}
template <int ROWB, bool WIN>
__device__ __forceinline__ void tile_load(__amdgpu_buffer_rsrc_t rw, unsigned off, u32x4 (&t)[8]) {
#pragma unroll
  for (int i = 0; i < 8; ++i) {
    if (WIN && (i & 1) == 0) window<TWIN>();
    t[i] = __builtin_amdgcn_raw_buffer_load_b128(rw, off, i * 4 * ROWB, WAUX);
  }
}
__device__ __forceinline__ void tile_store(float* tile, int gw, int r, int c, const u32x4 (&t)[8]) {
#pragma unroll
  for (int i = 0; i < 8; ++i) *reinterpret_cast<u32x4*>(tile + (gw * 32 + i * 4 + r) * TROW + c * 4) = t[i];
}
__device__ __forceinline__ void tile_frags(const float* tile, int gw, int r, int c, unsigned (&raw)[32]) {
#pragma unroll
  for (int i = 0; i < 32; ++i) raw[i] = __float_as_uint(tile[((i >> 3) * 32 + r * 8 + (i & 7)) * TROW + gw * 16 + c]);
}
__device__ __forceinline__ void act_frags(const float* act, int r, int c, u32x4 (&af)[4]) {
#pragma unroll
  for (int kk = 0; kk < 4; ++kk) {
    af[kk] = *reinterpret_cast<const u32x4*>(act + (c & 7) * 64 + kk * 16 + r * 4);
    if (c >= 8) af[kk] = u32x4{0u, 0u, 0u, 0u};
  }
}
__device__ __forceinline__ void pair_tile(const unsigned (&raw)[32], u32x4 (&bf)[8]) {
#pragma unroll
  for (int kk = 0; kk < 4; ++kk)
#pragma unroll
    for (int e = 0; e < 4; ++e) {
      bf[kk * 2][e] = perm_lo(raw[kk * 8 + 2 * e + 1], raw[kk * 8 + 2 * e]);
      bf[kk * 2 + 1][e] = perm_hi(raw[kk * 8 + 2 * e + 1], raw[kk * 8 + 2 * e]);
    }
}
__device__ __forceinline__ void mfma_tile(const u32x4 (&af)[4], const u32x4 (&bf)[8], f32x4& d0, f32x4& d1) {
#pragma unroll
  for (int kk = 0; kk < 4; ++kk) {
    d0 = __builtin_amdgcn_mfma_f32_16x16x32_bf16(__builtin_bit_cast(bf16x8, af[kk]), __builtin_bit_cast(bf16x8, bf[kk * 2]), d0, 0, 0, 0);
    d1 = __builtin_amdgcn_mfma_f32_16x16x32_bf16(__builtin_bit_cast(bf16x8, af[kk]), __builtin_bit_cast(bf16x8, bf[kk * 2 + 1]), d1, 0, 0, 0);
  }
}
template <int SLEEP, bool NARROW>
__device__ __forceinline__ void sweep8(__amdgpu_buffer_rsrc_t rs, unsigned off0, unsigned shi, unsigned slo, unsigned tag,
                                       unsigned pend, unsigned (&v)[16]) {
#pragma unroll
  for (int j = 0; j < 16; ++j) v[j] = 0u;
  unsigned act = pend;
  for (unsigned spins = 0; spins < SPIN_MAX; ++spins) {
    u32x4 g[8];
#pragma unroll
    for (int j = 0; j < 8; ++j) {
      const unsigned off = ((act >> j) & 1u) ? off0 + (j >> 1) * shi + (j & 1) * slo : OOB;
      g[j] = __builtin_amdgcn_raw_buffer_load_b128(rs, off, 0, 16);
    }
    __builtin_amdgcn_sched_barrier(0);
    const unsigned before = pend;
#pragma unroll
    for (int j = 0; j < 8; ++j) {
      const bool ok = ((act >> j) & 1u) && g[j][1] == tag && g[j][3] == tag;
      v[2 * j] = ok ? g[j][0] : v[2 * j];
      v[2 * j + 1] = ok ? g[j][2] : v[2 * j + 1];
      pend = ok ? (pend & ~(1u << j)) : pend;
    }
    if (!__any(pend != 0u)) break;
    if (NARROW) {
      const bool was_narrow_hit = __any(pend != before && act != before);
      act = was_narrow_hit ? pend : (pend & (0u - pend));
      if (!was_narrow_hit) __builtin_amdgcn_s_sleep(SLEEP);
    } else {
      act = pend;
      __builtin_amdgcn_s_sleep(SLEEP);
    }
  }
}
__device__ __forceinline__ void put_granule(gu64* g, unsigned tag, unsigned val) {
  __hip_atomic_store(g, ((unsigned long long)tag << 32) | val, RLX_AGENT);
}
__device__ __forceinline__ void put_rows(__amdgpu_buffer_rsrc_t rs, unsigned off, unsigned rstride, unsigned tag, int r,
                                         const f32x4& d0, const f32x4& d1) {
  if (r < 2) {
#pragma unroll
    for (int i = 0; i < 4; ++i)
      __builtin_amdgcn_raw_buffer_store_b128(u32x4{__float_as_uint(d0[i]), tag, __float_as_uint(d1[i]), tag}, rs,
                                             off + (r * 4 + i) * rstride, 0, 16);
  }
}
__device__ __forceinline__ void lds_arrive(unsigned* cnt, int lane) {
  asm volatile("s_waitcnt lgkmcnt(0)" ::: "memory");
  if (lane == 0) __hip_atomic_fetch_add(cnt, 1u, __ATOMIC_RELAXED, __HIP_MEMORY_SCOPE_WORKGROUP);
}
template <int SLEEP>
__device__ __forceinline__ void lds_wait(unsigned* cnt, unsigned n) {
  for (unsigned spins = 0; spins < 50u * SPIN_MAX; ++spins) {
    if (__hip_atomic_load(cnt, __ATOMIC_RELAXED, __HIP_MEMORY_SCOPE_WORKGROUP) >= n) break;
    __builtin_amdgcn_s_sleep(SLEEP);
  }
  asm volatile("" ::: "memory");
}

template <bool PF>
__device__ __forceinline__ void attn_step(u32x4 (&Kb)[NL], u32x4 (&Vb)[NL], __amdgpu_buffer_rsrc_t rsk, __amdgpu_buffer_rsrc_t rsv, unsigned so, unsigned vo,
                                          const u32x4& q, float& m, float& l, float (&acc)[8]) {
  float s[NL];
#pragma unroll
  for (int i = 0; i < NL; ++i) {
    float d = dot2(Kb[i][0], q[0], 0.f);
    d = dot2(Kb[i][1], q[1], d); d = dot2(Kb[i][2], q[2], d); d = dot2(Kb[i][3], q[3], d);
    s[i] = d;
  }
  if (PF) {
#pragma unroll
    for (int i = 0; i < NL; ++i) Kb[i] = ldkv<KAUX>(rsk, vo, so, i);
  }
  __builtin_amdgcn_sched_barrier(0);
  constexpr float SCALE = 0.08838834764831845f, LOG2E = 1.4426950408889634f;
#pragma unroll
  for (int i = 0; i < NL; ++i) s[i] = row_sum(s[i]);
#pragma unroll
  for (int i = 0; i < NL; i += 2) {
    unsigned u = pack_bf16(s[i], s[i + 1]);
    u = pack_bf16(bf_lo(u) * SCALE, bf_hi(u) * SCALE);
    s[i] = bf_lo(u) * LOG2E; s[i + 1] = bf_hi(u) * LOG2E;
  }
  float mn = m;
#pragma unroll
  for (int i = 0; i < NL; ++i) mn = fmaxf(mn, s[i]);
  const float alpha = __builtin_amdgcn_exp2f(m - mn);
  m = mn;
  float ps = 0.f;
#pragma unroll
  for (int i = 0; i < NL; ++i) { s[i] = __builtin_amdgcn_exp2f(s[i] - mn); ps += s[i]; }
  l = fmaf(l, alpha, ps);
#pragma unroll
  for (int k = 0; k < 8; ++k) acc[k] *= alpha;
#pragma unroll
  for (int i = 0; i < NL; ++i) {
#pragma unroll
    for (int j = 0; j < 4; ++j) {
      acc[2 * j] = fmaf(s[i], bf_lo(Vb[i][j]), acc[2 * j]);
      acc[2 * j + 1] = fmaf(s[i], bf_hi(Vb[i][j]), acc[2 * j + 1]);
    }
  }
  if (PF) {
#pragma unroll
    for (int i = 0; i < NL; ++i) Vb[i] = ldkv<VAUX>(rsv, vo, so, i);
  }
  __builtin_amdgcn_sched_barrier(0);
}

__device__ __forceinline__ void q_phase(const Params& p, float* lds, __amdgpu_buffer_rsrc_t rs, int h, int b, int hw, int lane) {
  unsigned* cnt = reinterpret_cast<unsigned*>(lds) + L_CNT;
  const int r = lane >> 4, c = lane & 15;
  u32x4 bf[8], af[4];
  unsigned raw[32];
  f32x4 d0 = {0.f, 0.f, 0.f, 0.f}, d1 = {0.f, 0.f, 0.f, 0.f};
  lds_wait<1>(cnt + C_T0, 4);
  tile_frags(lds + L_T, hw, r, c, raw);
  pair_tile(raw, bf);
  lds_wait<1>(cnt + C_XN, NHW);
  const unsigned tag = cnt[C_TAG];
  act_frags(lds + L_XN, r, c, af);
  mfma_tile(af, bf, d0, d1);
  const unsigned pbase = (unsigned)WS_QG + (unsigned)h * (64 * 1024);
  put_rows(rs, pbase + (unsigned)(b * 128 + hw * 32 + c * 2) * 8u, 8 * 1024, tag, r, d0, d1);
  if (hw == 0) {
    unsigned gr[16];
    sweep8<1, false>(rs, pbase + (unsigned)b * (8 * 1024) + (unsigned)lane * 16u, 2048, 1024, tag, 0xffu, gr);
    float t0 = 0.f, t1 = 0.f;
#pragma unroll
    for (int j = 0; j < 8; ++j) { t0 += __uint_as_float(gr[2 * j]); t1 += __uint_as_float(gr[2 * j + 1]); }
    reinterpret_cast<unsigned*>(lds)[L_Q + lane] = pack_bf16(t0, t1);
    lds_arrive(cnt + C_Q, lane);
  }
}

__global__ void __launch_bounds__(NT, NT / 256) fused_decode_kernel(Params p) {
  __shared__ __attribute__((aligned(16))) float lds[L_END];
  unsigned* ldsu = reinterpret_cast<unsigned*>(lds);
  const int bid = blockIdx.x, xcd = bid & 7, idx = bid >> 3;
  const int h = xcd * 4 + (idx >> 3), b = idx & 7;
  const int tid = threadIdx.x, lane = tid & 63, wave = __builtin_amdgcn_readfirstlane(tid >> 6);
  const int r = lane >> 4, c = lane & 15;
  const bool streamer = wave >= NHW;
  if (wave == 0) STAMP(0);

  if (streamer) {
    const int sw = wave - NHW;
    const size_t slab = (size_t)(b * NH + h) * SK * DH;
    const unsigned vo = (unsigned)lane * 16u;
    constexpr int STRIDE = NSW * CHK * DH * 2;
    constexpr int XB = CFG_XB;
    u32x4 Kb[NL], Vb[NL], Kx[XB][NL], Vx[XB][NL];
    __syncthreads();
#pragma nounroll
    for (int pass = ((DRY & 1) && sw == NSW - 1) ? 0 : 1; pass < 2; ++pass) {
      int real = __builtin_amdgcn_readfirstlane(pass);
      asm volatile("" : "+s"(real));
      const __amdgpu_buffer_rsrc_t rsk = __builtin_amdgcn_make_buffer_rsrc((void*)(p.kc + slab), 0, real ? SK * DH * 2 : 0, 0x00020000);
      const __amdgpu_buffer_rsrc_t rsv = __builtin_amdgcn_make_buffer_rsrc((void*)(p.vc + slab), 0, real ? SK * DH * 2 : 0, 0x00020000);
      unsigned so = (unsigned)sw * (CHK * DH * 2);
#pragma unroll
      for (int i = 0; i < NL; ++i) Kb[i] = ldkv<KAUX>(rsk, vo, so, i);
#pragma unroll
      for (int i = 0; i < NL; ++i) Vb[i] = ldkv<VAUX>(rsv, vo, so, i);
#pragma unroll
      for (int x = 0; x < XB; ++x) {
        window<NL>();
#pragma unroll
        for (int i = 0; i < NL; ++i) Kx[x][i] = ldkv<KAUX>(rsk, vo, so + (NCH - 1 - x) * STRIDE, i);
        window<NL>();
#pragma unroll
        for (int i = 0; i < NL; ++i) Vx[x][i] = ldkv<VAUX>(rsv, vo, so + (NCH - 1 - x) * STRIDE, i);
      }
      __builtin_amdgcn_sched_barrier(0);
      lds_wait<2>(ldsu + L_CNT + C_Q, real);
      if (sw == 0) STAMP(2);
      const unsigned tag = ldsu[L_CNT + C_TAG];
      const u32x4 q = *reinterpret_cast<const u32x4*>(lds + L_Q + c * 4);
      float m = -INFINITY, l = 0.f, acc[8];
#pragma unroll
      for (int k = 0; k < 8; ++k) acc[k] = 0.f;
      so += STRIDE;
      attn_step<true>(Kb, Vb, rsk, rsv, so, vo, q, m, l, acc);
#pragma unroll
      for (int x = 0; x < XB; ++x) attn_step<false>(Kx[x], Vx[x], rsk, rsv, so, vo, q, m, l, acc);
      const int nit = real ? NCH - 1 - XB : 2;
#pragma nounroll
      for (int it = 1; it < nit; ++it) {
        so += STRIDE;
        attn_step<true>(Kb, Vb, rsk, rsv, so, vo, q, m, l, acc);
      }
      attn_step<false>(Kb, Vb, rsk, rsv, so, vo, q, m, l, acc);
      STAMP(16 + sw);
      const float M = xrow_max(m);
      const float wt = __builtin_amdgcn_exp2f(m - M);
      l = xrow_sum(l * wt);
#pragma unroll
      for (int k = 0; k < 8; ++k) acc[k] = xrow_sum(acc[k] * wt);
      float* rec = lds + L_REC + sw * REC;
      if (r == 0) {
        f32x4* d = reinterpret_cast<f32x4*>(rec + 4 + c * 8);
        d[0] = f32x4{acc[0], acc[1], acc[2], acc[3]}; d[1] = f32x4{acc[4], acc[5], acc[6], acc[7]};
        if (c == 0) { rec[0] = M; rec[1] = l; }
      }
      unsigned arrived = 0;
      if (lane == 0) arrived = __hip_atomic_fetch_add(ldsu + L_CNT + (real ? C_STREAM : C_DRY0), 1u, __ATOMIC_ACQ_REL, __HIP_MEMORY_SCOPE_WORKGROUP);
      arrived = __builtin_amdgcn_readfirstlane(arrived);
      if (arrived == (real ? NSW - 1 : 0)) {
        __builtin_amdgcn_fence(__ATOMIC_ACQUIRE, "workgroup");
        float MM = -INFINITY;
#pragma unroll
        for (int w = 0; w < NSW; ++w) MM = fmaxf(MM, lds[L_REC + w * REC]);
        float L = 0.f, o0 = 0.f, o1 = 0.f;
#pragma unroll
        for (int w = 0; w < NSW; ++w) {
          const float* rw = lds + L_REC + w * REC;
          const float e = __builtin_amdgcn_exp2f(rw[0] - MM);
          const f32x2 a = *reinterpret_cast<const f32x2*>(rw + 4 + 2 * lane);
          L = fmaf(e, rw[1], L); o0 = fmaf(e, a[0], o0); o1 = fmaf(e, a[1], o1);
        }
        const float inv = 1.f / L;
        const unsigned pay = pack_bf16(o0 * inv, o1 * inv);
        const __amdgpu_buffer_rsrc_t rso = __builtin_amdgcn_make_buffer_rsrc(p.ws, 0, real ? (int)WS_END : 0, 0x00020000);
        __builtin_amdgcn_raw_buffer_store_b64(u32x2{pay, tag}, rso, (unsigned)WS_OG + (unsigned)((h * NB + b) * 64 + lane) * 8u, 0, 16);
        STAMP(3);
      }
    }
    return;
  }

  const int hw = wave, gw = wave & 3;
  const bool grpA = hw < 4;
  __builtin_amdgcn_s_setprio(3);
  unsigned* cnt = ldsu + L_CNT;
  const __amdgpu_buffer_rsrc_t rs = __builtin_amdgcn_make_buffer_rsrc(p.ws, 0, (int)WS_END, 0x00020000);
  const bool mlp = idx < 24 || ((idx < 28) == ((xcd & 1) == 0));
  const int g = idx < 24 ? xcd * 3 + (idx >> 3) : 24 + (xcd >> 1);

  const unsigned short* xr = p.x + hw * DM + lane * 16;
  const u32x4 x0 = ld16(xr), x1 = ld16(xr + 8);
  unsigned g0 = 0;
  g0 = __hip_atomic_load((gu32*)(p.ws + WS_G), RLX_AGENT);
  u32x4 ta[8], tb[8];
  if (grpA) {
    const __amdgpu_buffer_rsrc_t r0 = __builtin_amdgcn_make_buffer_rsrc((void*)p.wq, 0, DM * HD * 2, 0x00020000);
    tile_load<HD * 2, false>(r0, (unsigned)((b * 128 + gw * 32 + r) * HD + h * 128 + c * 8) * 2u, ta);
  }
  if (tid < 16) cnt[tid] = 0u;
  __builtin_amdgcn_sched_barrier(0);
  __syncthreads();
  {
    float v[16];
    unpack16(x0, x1, v);
    const LnStat s1 = ln_stat(v);
    u32x4 y0, y1;
#pragma unroll
    for (int j = 0; j < 4; ++j) {
      y0[j] = pack_bf16(ln_apply(v[2 * j], s1), ln_apply(v[2 * j + 1], s1));
      y1[j] = pack_bf16(ln_apply(v[8 + 2 * j], s1), ln_apply(v[8 + 2 * j + 1], s1));
    }
    if ((lane >> 3) == b) {
      u32x4* xd = reinterpret_cast<u32x4*>(lds + L_XN + hw * 64 + (lane & 7) * 8);
      xd[0] = y0; xd[1] = y1;
    }
    if (tid == 0) cnt[C_TAG] = g0 + 1u;
    lds_arrive(cnt + C_XN, lane);
    if (grpA) {
      tile_store(lds + L_T, gw, r, c, ta);
      lds_arrive(cnt + C_T0, lane);
      q_phase(p, lds, rs, h, b, hw, lane);
      const __amdgpu_buffer_rsrc_t r1 = __builtin_amdgcn_make_buffer_rsrc((void*)p.w2, 0, mlp ? FF * DM * 2 : 0, 0x00020000);
      tile_load<DM * 2, true>(r1, (unsigned)((g * 128 + gw * 32 + r) * DM + b * 128 + c * 8) * 2u, tb);
    }
    __builtin_amdgcn_sched_barrier(0);
    unpack16(y0, y1, v);
    const LnStat s2 = ln_stat(v);
#pragma unroll
    for (int j = 0; j < 4; ++j) {
      y0[j] = pack_bf16(ln_apply(v[2 * j], s2), ln_apply(v[2 * j + 1], s2));
      y1[j] = pack_bf16(ln_apply(v[8 + 2 * j], s2), ln_apply(v[8 + 2 * j + 1], s2));
    }
    if ((lane >> 3) == b) {
      u32x4* yd = reinterpret_cast<u32x4*>(lds + L_Y + hw * 64 + (lane & 7) * 8);
      yd[0] = y0; yd[1] = y1;
    }
    lds_arrive(cnt + C_Y, lane);
    if (!grpA) {
      const __amdgpu_buffer_rsrc_t r0 = __builtin_amdgcn_make_buffer_rsrc((void*)p.w1, 0, mlp ? DM * FF * 2 : 0, 0x00020000);
      tile_load<FF * 2, true>(r0, (unsigned)((b * 128 + gw * 32 + r) * FF + g * 128 + c * 8) * 2u, ta);
      const __amdgpu_buffer_rsrc_t r1 = __builtin_amdgcn_make_buffer_rsrc((void*)p.wo, 0, HD * DM * 2, 0x00020000);
      tile_load<DM * 2, true>(r1, (unsigned)((h * 128 + gw * 32 + r) * DM + b * 128 + c * 8) * 2u, tb);
      tile_store(lds + L_T + TILE, gw, r, c, ta);
      lds_arrive(cnt + C_T1, lane);
    }
  }
  u32x4 bf[8], af[4];
  unsigned raw[32], gr[16];
  f32x4 d0 = {0.f, 0.f, 0.f, 0.f}, d1 = {0.f, 0.f, 0.f, 0.f};
  lds_wait<1>(cnt + C_XN, NHW);
  const unsigned tag = cnt[C_TAG];
  const unsigned pbase = (unsigned)WS_UG + (unsigned)g * (64 * 1024);
  if (!grpA) {
    lds_wait<2>(cnt + C_T1, 4);
    tile_frags(lds + L_T + TILE, gw, r, c, raw);
    pair_tile(raw, bf);
    lds_wait<2>(cnt + C_Y, NHW);
    act_frags(lds + L_Y, r, c, af);
    mfma_tile(af, bf, d0, d1);
    if (mlp) put_rows(rs, pbase + (unsigned)(b * 128 + gw * 32 + c * 2) * 8u, 8 * 1024, tag, r, d0, d1);
  }
  tile_store(lds + L_T + (grpA ? 2 : 3) * TILE, gw, r, c, tb);
  lds_arrive(cnt + (grpA ? C_T2 : C_T3), lane);
  if (hw == 4 && mlp) {
    const unsigned b1p = *reinterpret_cast<const unsigned*>(p.b1 + g * 128 + lane * 2);
    __builtin_amdgcn_s_sleep(64);
    sweep8<64, true>(rs, pbase + (unsigned)b * (8 * 1024) + (unsigned)lane * 16u, 2048, 1024, tag, 0xffu, gr);
    float t0 = 0.f, t1 = 0.f;
#pragma unroll
    for (int j = 0; j < 8; ++j) { t0 += __uint_as_float(gr[2 * j]); t1 += __uint_as_float(gr[2 * j + 1]); }
    t0 = bfr(bfr(t0) + bf_lo(b1p)); t1 = bfr(bfr(t1) + bf_hi(b1p));
    const float e0 = 0.5f * t0 * (1.f + tanhf(0.7978845608028654f * (t0 + 0.044715f * t0 * t0 * t0)));
    const float e1 = 0.5f * t1 * (1.f + tanhf(0.7978845608028654f * (t1 + 0.044715f * t1 * t1 * t1)));
    put_granule((gu64*)(p.ws + WS_HG) + (size_t)(g * NB + b) * 64 + lane, tag, pack_bf16(e0, e1));
    STAMP(4);
  }
  lds_wait<2>(cnt + (grpA ? C_T2 : C_T3), 4);
  tile_frags(lds + L_T + (grpA ? 2 : 3) * TILE, gw, r, c, raw);
  pair_tile(raw, bf);
#pragma unroll
  for (int k = 0; k < 8; ++k) asm volatile("" : "+v"(bf[k]));
  d0 = f32x4{0.f, 0.f, 0.f, 0.f}; d1 = f32x4{0.f, 0.f, 0.f, 0.f};
  f32x4* dx = reinterpret_cast<f32x4*>(lds + L_D + (gw * 64 + lane) * 8);
  if (grpA) {
    if (mlp) {
      if (hw == 0) {
        __builtin_amdgcn_s_sleep(127); __builtin_amdgcn_s_sleep(127);
        sweep8<127, true>(rs, (unsigned)WS_HG + (unsigned)g * 4096u + (unsigned)lane * 16u, 2048, 1024, tag, 0x0fu, gr);
#pragma unroll
        for (int j = 0; j < 4; ++j) *reinterpret_cast<u32x2*>(lds + L_H + j * 128 + lane * 2) = u32x2{gr[2 * j], gr[2 * j + 1]};
        lds_arrive(cnt + C_H, lane);
      }
      lds_wait<32>(cnt + C_H, 1);
      act_frags(lds + L_H, r, c, af);
      mfma_tile(af, bf, d0, d1);
    }
    dx[0] = d0; dx[1] = d1;
    lds_arrive(cnt + C_D, lane);
    STAMP(32 + hw);
    if (hw == 0) {
      const int ob = bid & 7, on = (bid >> 3) * 32 + c * 2;
      const unsigned xv = *reinterpret_cast<const unsigned*>(p.x + ob * DM + on);
      const unsigned bv = *reinterpret_cast<const unsigned*>(p.b2 + on);
      const unsigned poff = (unsigned)(WS_PG + ((size_t)((ob * 32 + (bid >> 3)) * NH + r * 8) * 32 + c * 2) * 8);
      if (DRY & 4) {
        const unsigned w0 = __builtin_amdgcn_raw_buffer_load_b32(rs, lane == 0 ? poff : OOB, 0, 16);
        const unsigned w1 = *reinterpret_cast<const volatile unsigned*>(p.out + ob * DM + (bid >> 3) * 32);
        asm volatile("" ::"v"(w0), "v"(w1));
      }
#pragma nounroll
      for (int pass = (DRY & 2) ? 0 : 1; pass < 2; ++pass) {
        int real = __builtin_amdgcn_readfirstlane(pass);
        asm volatile("" : "+s"(real));
        lds_wait<4>(cnt + C_O, real);
        sweep8<PSLEEP, true>(rs, poff, 512, 256, tag, real ? 0xffu : 0u, gr);
        float t0 = 0.f, t1 = 0.f;
#pragma unroll
        for (int j = 0; j < 8; ++j) { t0 += __uint_as_float(gr[2 * j]); t1 += __uint_as_float(gr[2 * j + 1]); }
        t0 = xrow_sum(t0) + (bf_lo(xv) + bf_lo(bv));
        t1 = xrow_sum(t1) + (bf_hi(xv) + bf_hi(bv));
        const __amdgpu_buffer_rsrc_t ro = __builtin_amdgcn_make_buffer_rsrc(p.out, 0, real ? NB * DM * 2 : 0, 0x00020000);
        __builtin_amdgcn_raw_buffer_store_b32(pack_bf16(t0, t1), ro, r == 0 ? (unsigned)(ob * DM + on) * 2u : OOB, 0, 0);
        __builtin_amdgcn_raw_buffer_store_b32(tag, rs, (real && bid == 0 && lane == 0) ? (unsigned)WS_G : OOB, 0, 16);
        STAMP(5);
      }
    }
  } else {
    lds_wait<32>(cnt + C_D, 4);
    const unsigned poff = (unsigned)WS_PG + (unsigned)(((b * 4 + gw) * NH + h) * 32 + c * 2) * 8u;
    if ((DRY & 4) && hw == 4) {
      const unsigned w0 = __builtin_amdgcn_raw_buffer_load_b32(rs, lane < 8 ? poff + (unsigned)lane * (32 * NH * 32 * 8) : OOB, 0, 16);
      asm volatile("" ::"v"(w0));
    }
#pragma nounroll
    for (int pass = ((DRY & 2) && hw == 4) ? 0 : 1; pass < 2; ++pass) {
      int real = __builtin_amdgcn_readfirstlane(pass);
      asm volatile("" : "+s"(real));
      d0 = dx[0]; d1 = dx[1];
      if (hw == 4) {
        lds_wait<4>(cnt + C_STREAM, real ? NSW : 0);
        sweep8<PSLEEP, true>(rs, (unsigned)WS_OG + (unsigned)h * 4096u + (unsigned)lane * 16u, 2048, 1024, tag, real ? 0x0fu : 0u, gr);
#pragma unroll
        for (int j = 0; j < 4; ++j) *reinterpret_cast<u32x2*>(lds + L_H + j * 128 + lane * 2) = u32x2{gr[2 * j], gr[2 * j + 1]};
        lds_arrive(cnt + (real ? C_O : C_DRY1), lane);
      } else {
        lds_wait<8>(cnt + C_STREAM, NSW);
      }
      lds_wait<1>(cnt + C_O, real);
      act_frags(lds + L_H, r, c, af);
      mfma_tile(af, bf, d0, d1);
      const __amdgpu_buffer_rsrc_t rp = __builtin_amdgcn_make_buffer_rsrc(p.ws, 0, real ? (int)WS_END : 0, 0x00020000);
      put_rows(rp, poff, 32 * NH * 32 * 8, tag, r, d0, d1);
      STAMP(40 + gw);
    }
  }
}

}

extern "C" void kernel_launch(void* const* d_in, const int* in_sizes, int n_in, void* d_out, int out_size, void* d_ws,
                              size_t ws_size, hipStream_t stream) {
  Params p;
  p.x = (const unsigned short*)d_in[0];
  p.kc = (const unsigned short*)d_in[1];
  p.vc = (const unsigned short*)d_in[2];
  p.wq = (const unsigned short*)d_in[3];
  p.wo = (const unsigned short*)d_in[4];
  p.w1 = (const unsigned short*)d_in[6];
  p.b1 = (const unsigned short*)d_in[7];
  p.w2 = (const unsigned short*)d_in[8];
  p.b2 = (const unsigned short*)d_in[9];
  p.out = (unsigned short*)d_out;
  p.ws = (unsigned char*)d_ws;
  fused_decode_kernel<<<dim3(NB * NH), dim3(NT), 0, stream>>>(p);
}
```

```cpp
#include <hip/hip_runtime.h>
#include <hip/hip_bf16.h>
#include <stdint.h>

#define MIMIC 1
#define USE_MEMSET 0

namespace {

constexpr int NB = 8, DM = 1024, NH = 32, SK = 4096, DH = 128, FF = 3584, HD = NH * DH;
#define CFG_NSW 8
#define CFG_NL 2
#define CFG_XB 3
#define CFG_DELAY 0
#define CFG_ROT 0
#define PACE_END 0
#define PACE_BEG 400
#define CFG_EB 2
#define CFG_EBS 0
#define XM 0
#define FB 1
#define FB_BASE 4
#define FB_MAX 16
#define FB_NUM 6
#define FB_N 8
#define FB_SH 9
#define MSH 1
#define CFG_WIN 1
#define TWIN 2
#define PSLEEP 4
#define KAUX 2
#define VAUX 2
#define WAUX 2
constexpr int NSW = CFG_NSW, NHW = 8, NT = (NSW + NHW) * 64;
constexpr int NL = CFG_NL;
constexpr int CHK = NL * 4;
constexpr int NCH = SK / CHK / NSW;
constexpr int XBE = CFG_XB + CFG_EB + (CFG_EB ? (CFG_EBS ? CFG_EB : 1) : 0);
constexpr unsigned SPIN_MAX = 200000u;

constexpr int NG = FF / 128;
constexpr size_t WS_G = 0;
constexpr size_t WS_OG = 4096;
constexpr size_t WS_HG = WS_OG + (size_t)NH * NB * 64 * 8;
constexpr size_t WS_QG = WS_HG + (size_t)NG * NB * 64 * 8;
constexpr size_t WS_UG = WS_QG + (size_t)NH * NB * NB * 128 * 8;
constexpr size_t WS_PG = WS_UG + (size_t)NG * NB * NB * 128 * 8;
constexpr size_t WS_PR = WS_PG + (size_t)NB * DM * NH * 8;
constexpr size_t WS_END = WS_PR + 2 * 256 * 128;

typedef __attribute__((ext_vector_type(2))) __bf16 bf16x2;
typedef __attribute__((ext_vector_type(2))) float f32x2;
typedef __attribute__((ext_vector_type(4))) float f32x4;
typedef __attribute__((ext_vector_type(4))) unsigned u32x4;
typedef __attribute__((ext_vector_type(2))) unsigned u32x2;
typedef __attribute__((ext_vector_type(8))) __bf16 bf16x8;
typedef __attribute__((address_space(1))) unsigned gu32;
typedef __attribute__((address_space(1))) unsigned long long gu64;
#define RLX_AGENT __ATOMIC_RELAXED, __HIP_MEMORY_SCOPE_AGENT

__device__ __forceinline__ float dot2(unsigned a, unsigned b, float acc) {
  return __builtin_amdgcn_fdot2_f32_bf16(__builtin_bit_cast(bf16x2, a), __builtin_bit_cast(bf16x2, b), acc, false);
}
__device__ __forceinline__ unsigned pack_bf16(float lo, float hi) {
  f32x2 v = {lo, hi};
  return __builtin_bit_cast(unsigned, __builtin_convertvector(v, bf16x2));
}
__device__ __forceinline__ float bf_lo(unsigned u) { return __builtin_bit_cast(float, u << 16); }
__device__ __forceinline__ float bf_hi(unsigned u) { return __builtin_bit_cast(float, u & 0xffff0000u); }
__device__ __forceinline__ float bfr(float x) { return bf_lo(pack_bf16(x, 0.f)); }
template <int CTRL> __device__ __forceinline__ float dpp(float x) {
  return __builtin_bit_cast(float, __builtin_amdgcn_mov_dpp(__builtin_bit_cast(int, x), CTRL, 0xf, 0xf, true));
}
constexpr int XOR1 = 0xB1, XOR2 = 0x4E, XOR7 = 0x141, XOR8 = 0x128;
__device__ __forceinline__ float row_sum(float x) {
  x += dpp<XOR1>(x); x += dpp<XOR2>(x); x += dpp<XOR7>(x); x += dpp<XOR8>(x); return x;
}
__device__ __forceinline__ float xrow_sum(float x) {
  auto s = __builtin_amdgcn_permlane16_swap(__float_as_uint(x), __float_as_uint(x), false, false);
  x = __uint_as_float(s[0]) + __uint_as_float(s[1]);
  auto t = __builtin_amdgcn_permlane32_swap(__float_as_uint(x), __float_as_uint(x), false, false);
  return __uint_as_float(t[0]) + __uint_as_float(t[1]);
}
__device__ __forceinline__ float xrow_max(float x) {
  auto s = __builtin_amdgcn_permlane16_swap(__float_as_uint(x), __float_as_uint(x), false, false);
  x = fmaxf(__uint_as_float(s[0]), __uint_as_float(s[1]));
  auto t = __builtin_amdgcn_permlane32_swap(__float_as_uint(x), __float_as_uint(x), false, false);
  return fmaxf(__uint_as_float(t[0]), __uint_as_float(t[1]));
}
__device__ __forceinline__ float wave_sum(float x) { return xrow_sum(row_sum(x)); }
__device__ __forceinline__ unsigned perm_lo(unsigned hi_src, unsigned lo_src) { return __builtin_amdgcn_perm(hi_src, lo_src, 0x05040100u); }
__device__ __forceinline__ unsigned perm_hi(unsigned hi_src, unsigned lo_src) { return __builtin_amdgcn_perm(hi_src, lo_src, 0x07060302u); }
__device__ __forceinline__ u32x4 ld16(const void* p) { return *reinterpret_cast<const u32x4*>(p); }
__device__ __forceinline__ u32x4 ld16nt(const void* p) { return __builtin_nontemporal_load(reinterpret_cast<const u32x4*>(p)); }

template <int AUX>
__device__ __forceinline__ u32x4 ldkv(__amdgpu_buffer_rsrc_t rs, unsigned vo, unsigned so, int i) {
  return __builtin_amdgcn_raw_buffer_load_b128(rs, vo + (i & 3) * 1024, so + (i >> 2) * 4096, AUX);
}

struct LnStat { float sub, den; };
__device__ __forceinline__ LnStat ln_stat(const float (&v)[16]) {
  float s = 0.f;
#pragma unroll
  for (int i = 0; i < 16; ++i) s += v[i];
  const float mean = wave_sum(s) * (1.f / DM);
  float q = 0.f;
#pragma unroll
  for (int i = 0; i < 16; ++i) { const float d = v[i] - mean; q = fmaf(d, d, q); }
  const float var = wave_sum(q) * (1.f / DM);
  LnStat st;
  st.sub = bfr(mean);
  st.den = bfr(sqrtf(bfr(bfr(var) + 1e-5f)));
  return st;
}
__device__ __forceinline__ float ln_apply(float x, LnStat st) {
  return bfr(x - st.sub) / st.den;
}
__device__ __forceinline__ void unpack16(u32x4 a, u32x4 b, float (&v)[16]) {
#pragma unroll
  for (int j = 0; j < 4; ++j) { v[2 * j] = bf_lo(a[j]); v[2 * j + 1] = bf_hi(a[j]); v[8 + 2 * j] = bf_lo(b[j]); v[8 + 2 * j + 1] = bf_hi(b[j]); }
}
#define STAMP(i) do {} while (0)

struct Params {
  const unsigned short* x; const unsigned short* kc; const unsigned short* vc; const unsigned short* wq; const unsigned short* wo;
  const unsigned short* w1; const unsigned short* b1; const unsigned short* w2; const unsigned short* b2;
  unsigned short* out; unsigned char* ws;
};

constexpr int TROW = 68;
constexpr int TILE = 128 * TROW;
constexpr int L_T = 0;
constexpr int L_Q = L_T + 4 * TILE;
constexpr int L_XN = L_Q + 64;
constexpr int L_Y = L_XN + 512;
constexpr int L_H = L_Y + 512;
constexpr int L_D = L_H + 512;
constexpr int L_REC = L_D + 2048;
constexpr int REC = 132;
constexpr int L_CNT = L_REC + NSW * REC;
constexpr int L_END = L_CNT + 16;
enum { C_STREAM = 0, C_XN, C_Y, C_TAG, C_Q, C_T0, C_T1, C_T2, C_T3, C_H, C_D, C_O, C_LV, C_PROG };
constexpr unsigned OOB = 0xfffffff0u;

template <int N> __device__ __forceinline__ void window() {
  __builtin_amdgcn_sched_barrier(0);
  asm volatile("s_waitcnt vmcnt(%0)" ::"i"(N) : "memory");
  __builtin_amdgcn_sched_barrier(0);
}
template <int ROWB, bool WIN>
__device__ __forceinline__ void tile_load(__amdgpu_buffer_rsrc_t rw, unsigned off, u32x4 (&t)[8]) {
#pragma unroll
  for (int i = 0; i < 8; ++i) {
    if (WIN && (i & 1) == 0) window<TWIN>();
    t[i] = __builtin_amdgcn_raw_buffer_load_b128(rw, off, i * 4 * ROWB, WAUX);
  }
}
__device__ __forceinline__ void tile_store(float* tile, int gw, int r, int c, const u32x4 (&t)[8]) {
#pragma unroll
  for (int i = 0; i < 8; ++i) *reinterpret_cast<u32x4*>(tile + (gw * 32 + i * 4 + r) * TROW + c * 4) = t[i];
}
__device__ __forceinline__ void tile_frags(const float* tile, int gw, int r, int c, unsigned (&raw)[32]) {
#pragma unroll
  for (int i = 0; i < 32; ++i) raw[i] = __float_as_uint(tile[((i >> 3) * 32 + r * 8 + (i & 7)) * TROW + gw * 16 + c]);
}
__device__ __forceinline__ void act_frags(const float* act, int r, int c, u32x4 (&af)[4]) {
#pragma unroll
  for (int kk = 0; kk < 4; ++kk) {
    af[kk] = *reinterpret_cast<const u32x4*>(act + (c & 7) * 64 + kk * 16 + r * 4);
    if (c >= 8) af[kk] = u32x4{0u, 0u, 0u, 0u};
  }
}
__device__ __forceinline__ void pair_tile(const unsigned (&raw)[32], u32x4 (&bf)[8]) {
#pragma unroll
  for (int kk = 0; kk < 4; ++kk)
#pragma unroll
    for (int e = 0; e < 4; ++e) {
      bf[kk * 2][e] = perm_lo(raw[kk * 8 + 2 * e + 1], raw[kk * 8 + 2 * e]);
      bf[kk * 2 + 1][e] = perm_hi(raw[kk * 8 + 2 * e + 1], raw[kk * 8 + 2 * e]);
    }
}
__device__ __forceinline__ void mfma_tile(const u32x4 (&af)[4], const u32x4 (&bf)[8], f32x4& d0, f32x4& d1) {
#pragma unroll
  for (int kk = 0; kk < 4; ++kk) {
    d0 = __builtin_amdgcn_mfma_f32_16x16x32_bf16(__builtin_bit_cast(bf16x8, af[kk]), __builtin_bit_cast(bf16x8, bf[kk * 2]), d0, 0, 0, 0);
    d1 = __builtin_amdgcn_mfma_f32_16x16x32_bf16(__builtin_bit_cast(bf16x8, af[kk]), __builtin_bit_cast(bf16x8, bf[kk * 2 + 1]), d1, 0, 0, 0);
  }
}
template <int SLEEP, bool NARROW>
__device__ __forceinline__ void sweep8(__amdgpu_buffer_rsrc_t rs, unsigned off0, unsigned shi, unsigned slo, unsigned tag,
                                       unsigned pend, unsigned (&v)[16]) {
#pragma unroll
  for (int j = 0; j < 16; ++j) v[j] = 0u;
  unsigned act = pend;
  for (unsigned spins = 0; spins < SPIN_MAX; ++spins) {
    u32x4 g[8];
#pragma unroll
    for (int j = 0; j < 8; ++j) {
      const unsigned off = ((act >> j) & 1u) ? off0 + (j >> 1) * shi + (j & 1) * slo : OOB;
      g[j] = __builtin_amdgcn_raw_buffer_load_b128(rs, off, 0, 16);
    }
    __builtin_amdgcn_sched_barrier(0);
    const unsigned before = pend;
#pragma unroll
    for (int j = 0; j < 8; ++j) {
      const bool ok = ((act >> j) & 1u) && g[j][1] == tag && g[j][3] == tag;
      v[2 * j] = ok ? g[j][0] : v[2 * j];
      v[2 * j + 1] = ok ? g[j][2] : v[2 * j + 1];
      pend = ok ? (pend & ~(1u << j)) : pend;
    }
    if (!__any(pend != 0u)) break;
    if (NARROW) {
      const bool was_narrow_hit = __any(pend != before && act != before);
      act = was_narrow_hit ? pend : (pend & (0u - pend));
      if (!was_narrow_hit) __builtin_amdgcn_s_sleep(SLEEP);
    } else {
      act = pend;
      __builtin_amdgcn_s_sleep(SLEEP);
    }
  }
}
__device__ __forceinline__ void put_granule(gu64* g, unsigned tag, unsigned val) {
  __hip_atomic_store(g, ((unsigned long long)tag << 32) | val, RLX_AGENT);
}
__device__ __forceinline__ void put_rows(__amdgpu_buffer_rsrc_t rs, unsigned off, unsigned rstride, unsigned tag, int r,
                                         const f32x4& d0, const f32x4& d1) {
  if (r < 2) {
#pragma unroll
    for (int i = 0; i < 4; ++i)
      __builtin_amdgcn_raw_buffer_store_b128(u32x4{__float_as_uint(d0[i]), tag, __float_as_uint(d1[i]), tag}, rs,
                                             off + (r * 4 + i) * rstride, 0, 16);
  }
}
__device__ __forceinline__ void lds_arrive(unsigned* cnt, int lane) {
  asm volatile("s_waitcnt lgkmcnt(0)" ::: "memory");
  if (lane == 0) __hip_atomic_fetch_add(cnt, 1u, __ATOMIC_RELAXED, __HIP_MEMORY_SCOPE_WORKGROUP);
}
template <int SLEEP>
__device__ __forceinline__ void lds_wait(unsigned* cnt, unsigned n) {
  for (unsigned spins = 0; spins < 50u * SPIN_MAX; ++spins) {
    if (__hip_atomic_load(cnt, __ATOMIC_RELAXED, __HIP_MEMORY_SCOPE_WORKGROUP) >= n) break;
    __builtin_amdgcn_s_sleep(SLEEP);
  }
  asm volatile("" ::: "memory");
}

__device__ __forceinline__ void hold(int lv) {
  __builtin_amdgcn_sched_barrier(0);
#pragma nounroll
  for (int n = lv; n > 0; --n) __builtin_amdgcn_s_sleep(1);
  __builtin_amdgcn_sched_barrier(0);
}
template <bool PF>
__device__ __forceinline__ void attn_step(u32x4 (&Kb)[NL], u32x4 (&Vb)[NL], __amdgpu_buffer_rsrc_t rsk, __amdgpu_buffer_rsrc_t rsv, unsigned so, unsigned vo,
                                          const u32x4& q, float& m, float& l, float (&acc)[8], int lv = 0) {
  float s[NL];
#pragma unroll
  for (int i = 0; i < NL; ++i) {
    float d = dot2(Kb[i][0], q[0], 0.f);
    d = dot2(Kb[i][1], q[1], d); d = dot2(Kb[i][2], q[2], d); d = dot2(Kb[i][3], q[3], d);
    s[i] = d;
  }
  if (PF) {
#pragma unroll
    for (int i = 0; i < NL; ++i) asm volatile("" : "+v"(s[i]));
    hold(lv);
#pragma unroll
    for (int i = 0; i < NL; ++i) Kb[i] = ldkv<KAUX>(rsk, vo, so, i);
  }
  __builtin_amdgcn_sched_barrier(0);
  constexpr float SCALE = 0.08838834764831845f, LOG2E = 1.4426950408889634f;
#pragma unroll
  for (int i = 0; i < NL; ++i) s[i] = row_sum(s[i]);
#pragma unroll
  for (int i = 0; i < NL; i += 2) {
    unsigned u = pack_bf16(s[i], s[i + 1]);
    u = pack_bf16(bf_lo(u) * SCALE, bf_hi(u) * SCALE);
    s[i] = bf_lo(u) * LOG2E; s[i + 1] = bf_hi(u) * LOG2E;
  }
  float mn = m;
#pragma unroll
  for (int i = 0; i < NL; ++i) mn = fmaxf(mn, s[i]);
  const float alpha = __builtin_amdgcn_exp2f(m - mn);
  m = mn;
  float ps = 0.f;
#pragma unroll
  for (int i = 0; i < NL; ++i) { s[i] = __builtin_amdgcn_exp2f(s[i] - mn); ps += s[i]; }
  l = fmaf(l, alpha, ps);
#pragma unroll
  for (int k = 0; k < 8; ++k) acc[k] *= alpha;
#pragma unroll
  for (int i = 0; i < NL; ++i) {
#pragma unroll
    for (int j = 0; j < 4; ++j) {
      acc[2 * j] = fmaf(s[i], bf_lo(Vb[i][j]), acc[2 * j]);
      acc[2 * j + 1] = fmaf(s[i], bf_hi(Vb[i][j]), acc[2 * j + 1]);
    }
  }
  if (PF) {
#pragma unroll
    for (int k = 0; k < 8; ++k) asm volatile("" : "+v"(acc[k]));
    hold(lv);
#pragma unroll
    for (int i = 0; i < NL; ++i) Vb[i] = ldkv<VAUX>(rsv, vo, so, i);
  }
  __builtin_amdgcn_sched_barrier(0);
}

__device__ __forceinline__ void q_phase(const Params& p, float* lds, __amdgpu_buffer_rsrc_t rs, int h, int b, int hw, int lane) {
  unsigned* cnt = reinterpret_cast<unsigned*>(lds) + L_CNT;
  const int r = lane >> 4, c = lane & 15;
  u32x4 bf[8], af[4];
  unsigned raw[32];
  f32x4 d0 = {0.f, 0.f, 0.f, 0.f}, d1 = {0.f, 0.f, 0.f, 0.f};
  lds_wait<1>(cnt + C_T0, 4);
  tile_frags(lds + L_T, hw, r, c, raw);
  pair_tile(raw, bf);
  lds_wait<1>(cnt + C_XN, NHW);
  const unsigned tag = cnt[C_TAG];
  act_frags(lds + L_XN, r, c, af);
  mfma_tile(af, bf, d0, d1);
  const unsigned pbase = (unsigned)WS_QG + (unsigned)h * (64 * 1024);
  put_rows(rs, pbase + (unsigned)(b * 128 + hw * 32 + c * 2) * 8u, 8 * 1024, tag, r, d0, d1);
  if (hw == 0) {
    unsigned gr[16];
    sweep8<1, false>(rs, pbase + (unsigned)b * (8 * 1024) + (unsigned)lane * 16u, 2048, 1024, tag, 0xffu, gr);
    float t0 = 0.f, t1 = 0.f;
#pragma unroll
    for (int j = 0; j < 8; ++j) { t0 += __uint_as_float(gr[2 * j]); t1 += __uint_as_float(gr[2 * j + 1]); }
    reinterpret_cast<unsigned*>(lds)[L_Q + lane] = pack_bf16(t0, t1);
    lds_arrive(cnt + C_Q, lane);
  }
}

__global__ void __launch_bounds__(NT, NT / 256) fused_decode_kernel(Params p) {
  __shared__ __attribute__((aligned(16))) float lds[L_END];
  unsigned* ldsu = reinterpret_cast<unsigned*>(lds);
  const int bid = blockIdx.x, xcd = bid & 7, idx = bid >> 3;
  const int h = xcd * 4 + (idx >> 3), b = idx & 7;
  const int tid = threadIdx.x, lane = tid & 63, wave = __builtin_amdgcn_readfirstlane(tid >> 6);
  const int r = lane >> 4, c = lane & 15;
  const bool streamer = wave >= NHW;
  if (wave == 0) STAMP(0);

  if (streamer) {
    const int sw = wave - NHW;
    const size_t slab = (size_t)(b * NH + h) * SK * DH;
    const __amdgpu_buffer_rsrc_t rsk = __builtin_amdgcn_make_buffer_rsrc((void*)(p.kc + slab), 0, SK * DH * 2, 0x00020000);
    const __amdgpu_buffer_rsrc_t rsv = __builtin_amdgcn_make_buffer_rsrc((void*)(p.vc + slab), 0, SK * DH * 2, 0x00020000);
    unsigned so = (unsigned)sw * (CHK * DH * 2);
    const unsigned vo = (unsigned)lane * 16u;
    constexpr int STRIDE = NSW * CHK * DH * 2;
    constexpr int XB = CFG_XB;
    u32x4 Kb[NL], Vb[NL], Kx[XB > 0 ? XB : 1][NL], Vx[XB > 0 ? XB : 1][NL];
    __syncthreads();
    if (CFG_DELAY) __builtin_amdgcn_s_sleep(CFG_DELAY);
#pragma unroll
    for (int i = 0; i < NL; ++i) Kb[i] = ldkv<KAUX>(rsk, vo, so, i);
#pragma unroll
    for (int i = 0; i < NL; ++i) Vb[i] = ldkv<VAUX>(rsv, vo, so, i);
#pragma unroll
    for (int x = 0; x < XB; ++x) {
      window<NL>();
#pragma unroll
      for (int i = 0; i < NL; ++i) Kx[x][i] = ldkv<KAUX>(rsk, vo, so + (NCH - 1 - x) * STRIDE, i);
      window<NL>();
#pragma unroll
      for (int i = 0; i < NL; ++i) Vx[x][i] = ldkv<VAUX>(rsv, vo, so + (NCH - 1 - x) * STRIDE, i);
    }
    __builtin_amdgcn_sched_barrier(0);
    lds_wait<2>(ldsu + L_CNT + C_Q, 1);
    if (sw == 0) STAMP(2);
    const unsigned tag = ldsu[L_CNT + C_TAG];
    const u32x4 q = *reinterpret_cast<const u32x4*>(lds + L_Q + c * 4);
    float m = -INFINITY, l = 0.f, acc[8];
#pragma unroll
    for (int k = 0; k < 8; ++k) acc[k] = 0.f;
    if (XB > 0) {
      so += STRIDE;
      attn_step<true>(Kb, Vb, rsk, rsv, so, vo, q, m, l, acc);
#pragma unroll
      for (int x = 0; x < XB; ++x) attn_step<false>(Kx[x], Vx[x], rsk, rsv, so, vo, q, m, l, acc);
    }
    constexpr int EB = CFG_EB;
    const unsigned eo = (unsigned)sw * (CHK * DH * 2) + (NCH - 1 - XB) * STRIDE;
    for (int it = 1; it < NCH - 1 - XB - EB - (EB ? (CFG_EBS ? EB : 1) : 0); ++it) {
      so += STRIDE;
      const int lv = __builtin_amdgcn_readfirstlane(__hip_atomic_load(ldsu + L_CNT + C_LV, __ATOMIC_RELAXED, __HIP_MEMORY_SCOPE_WORKGROUP));
      if (FB && lane == 0) __hip_atomic_fetch_add(ldsu + L_CNT + C_PROG, 1u, __ATOMIC_RELAXED, __HIP_MEMORY_SCOPE_WORKGROUP);
      attn_step<true>(Kb, Vb, rsk, rsv, so, vo, q, m, l, acc, lv);
    }
    if (EB) {
#pragma unroll
      for (int x = 0; x < EB; ++x) {
#pragma unroll
        for (int i = 0; i < NL; ++i) Kx[x][i] = ldkv<KAUX>(rsk, vo, eo - x * STRIDE, i);
#pragma unroll
        for (int i = 0; i < NL; ++i) Vx[x][i] = ldkv<VAUX>(rsv, vo, eo - x * STRIDE, i);
        __builtin_amdgcn_sched_barrier(0);
        if (CFG_EBS || x == EB - 1) {
          so += STRIDE;
          attn_step<true>(Kb, Vb, rsk, rsv, so, vo, q, m, l, acc);
        }
      }
#pragma unroll
      for (int x = 0; x < EB; ++x) attn_step<false>(Kx[x], Vx[x], rsk, rsv, so, vo, q, m, l, acc);
    }
    attn_step<false>(Kb, Vb, rsk, rsv, so, vo, q, m, l, acc);
    STAMP(16 + sw);
    const float M = xrow_max(m);
    const float wt = __builtin_amdgcn_exp2f(m - M);
    l = xrow_sum(l * wt);
#pragma unroll
    for (int k = 0; k < 8; ++k) acc[k] = xrow_sum(acc[k] * wt);
    float* rec = lds + L_REC + sw * REC;
    if (r == 0) {
      f32x4* d = reinterpret_cast<f32x4*>(rec + 4 + c * 8);
      d[0] = f32x4{acc[0], acc[1], acc[2], acc[3]}; d[1] = f32x4{acc[4], acc[5], acc[6], acc[7]};
      if (c == 0) { rec[0] = M; rec[1] = l; }
    }
    unsigned arrived = 0;
    if (lane == 0) arrived = __hip_atomic_fetch_add(ldsu + L_CNT + C_STREAM, 1u, __ATOMIC_ACQ_REL, __HIP_MEMORY_SCOPE_WORKGROUP);
    arrived = __builtin_amdgcn_readfirstlane(arrived);
    if (arrived == NSW - 1) {
      __builtin_amdgcn_fence(__ATOMIC_ACQUIRE, "workgroup");
      float MM = -INFINITY;
#pragma unroll
      for (int w = 0; w < NSW; ++w) MM = fmaxf(MM, lds[L_REC + w * REC]);
      float L = 0.f, o0 = 0.f, o1 = 0.f;
#pragma unroll
      for (int w = 0; w < NSW; ++w) {
        const float* rw = lds + L_REC + w * REC;
        const float e = __builtin_amdgcn_exp2f(rw[0] - MM);
        const f32x2 a = *reinterpret_cast<const f32x2*>(rw + 4 + 2 * lane);
        L = fmaf(e, rw[1], L); o0 = fmaf(e, a[0], o0); o1 = fmaf(e, a[1], o1);
      }
      const float inv = 1.f / L;
      const unsigned pay = pack_bf16(o0 * inv, o1 * inv);
      gu64* og = (gu64*)(p.ws + WS_OG) + (size_t)(h * NB + b) * 64 + lane;
      __hip_atomic_store(og, ((unsigned long long)tag << 32) | pay, RLX_AGENT);
      STAMP(3);
    }
    return;
  }

  const int hw = wave, gw = wave & 3;
  const bool grpA = hw < 4;
  __builtin_amdgcn_s_setprio(3);
  unsigned* cnt = ldsu + L_CNT;
  const __amdgpu_buffer_rsrc_t rs = __builtin_amdgcn_make_buffer_rsrc(p.ws, 0, (int)WS_END, 0x00020000);
  const bool mlp = idx < 24 || (xcd & 1) == 0;
  const int g = idx < 24 ? xcd * 3 + (idx >> 3) : 24 + (xcd >> 1);

  const unsigned short* xr = p.x + hw * DM + lane * 16;
  const u32x4 x0 = ld16(xr), x1 = ld16(xr + 8);
  unsigned g0 = 0;
  g0 = __hip_atomic_load((gu32*)(p.ws + WS_G), RLX_AGENT);
  u32x4 ta[8], tb[8];
  if (grpA) {
    const __amdgpu_buffer_rsrc_t r0 = __builtin_amdgcn_make_buffer_rsrc((void*)p.wq, 0, DM * HD * 2, 0x00020000);
    tile_load<HD * 2, false>(r0, (unsigned)((b * 128 + gw * 32 + r) * HD + h * 128 + c * 8) * 2u, ta);
  }
  const int lv0 = (xcd & 1) ? 0 : FB_BASE;
  if (tid < 16) cnt[tid] = tid == C_LV ? lv0 : 0u;
  __builtin_amdgcn_sched_barrier(0);
  __syncthreads();
  {
    float v[16];
    unpack16(x0, x1, v);
    const LnStat s1 = ln_stat(v);
    u32x4 y0, y1;
#pragma unroll
    for (int j = 0; j < 4; ++j) {
      y0[j] = pack_bf16(ln_apply(v[2 * j], s1), ln_apply(v[2 * j + 1], s1));
      y1[j] = pack_bf16(ln_apply(v[8 + 2 * j], s1), ln_apply(v[8 + 2 * j + 1], s1));
    }
    if ((lane >> 3) == b) {
      u32x4* xd = reinterpret_cast<u32x4*>(lds + L_XN + hw * 64 + (lane & 7) * 8);
      xd[0] = y0; xd[1] = y1;
    }
    if (tid == 0) cnt[C_TAG] = g0 + 1u;
    lds_arrive(cnt + C_XN, lane);
    if (grpA) {
      tile_store(lds + L_T, gw, r, c, ta);
      lds_arrive(cnt + C_T0, lane);
      q_phase(p, lds, rs, h, b, hw, lane);
      const __amdgpu_buffer_rsrc_t r1 = __builtin_amdgcn_make_buffer_rsrc((void*)p.w2, 0, mlp ? FF * DM * 2 : 0, 0x00020000);
      tile_load<DM * 2, true>(r1, (unsigned)((g * 128 + gw * 32 + r) * DM + b * 128 + c * 8) * 2u, tb);
    }
    __builtin_amdgcn_sched_barrier(0);
    unpack16(y0, y1, v);
    const LnStat s2 = ln_stat(v);
#pragma unroll
    for (int j = 0; j < 4; ++j) {
      y0[j] = pack_bf16(ln_apply(v[2 * j], s2), ln_apply(v[2 * j + 1], s2));
      y1[j] = pack_bf16(ln_apply(v[8 + 2 * j], s2), ln_apply(v[8 + 2 * j + 1], s2));
    }
    if ((lane >> 3) == b) {
      u32x4* yd = reinterpret_cast<u32x4*>(lds + L_Y + hw * 64 + (lane & 7) * 8);
      yd[0] = y0; yd[1] = y1;
    }
    lds_arrive(cnt + C_Y, lane);
    if (!grpA) {
      const __amdgpu_buffer_rsrc_t r0 = __builtin_amdgcn_make_buffer_rsrc((void*)p.w1, 0, mlp ? DM * FF * 2 : 0, 0x00020000);
      tile_load<FF * 2, true>(r0, (unsigned)((b * 128 + gw * 32 + r) * FF + g * 128 + c * 8) * 2u, ta);
      const __amdgpu_buffer_rsrc_t r1 = __builtin_amdgcn_make_buffer_rsrc((void*)p.wo, 0, HD * DM * 2, 0x00020000);
      tile_load<DM * 2, true>(r1, (unsigned)((h * 128 + gw * 32 + r) * DM + b * 128 + c * 8) * 2u, tb);
      tile_store(lds + L_T + TILE, gw, r, c, ta);
      lds_arrive(cnt + C_T1, lane);
    }
  }
  u32x4 bf[8], af[4];
  unsigned raw[32], gr[16];
  f32x4 d0 = {0.f, 0.f, 0.f, 0.f}, d1 = {0.f, 0.f, 0.f, 0.f};
  lds_wait<1>(cnt + C_XN, NHW);
  const unsigned tag = cnt[C_TAG];
  const unsigned pbase = (unsigned)WS_UG + (unsigned)g * (64 * 1024);
  if (!grpA) {
    lds_wait<2>(cnt + C_T1, 4);
    tile_frags(lds + L_T + TILE, gw, r, c, raw);
    pair_tile(raw, bf);
    lds_wait<2>(cnt + C_Y, NHW);
    act_frags(lds + L_Y, r, c, af);
    mfma_tile(af, bf, d0, d1);
    if (mlp) put_rows(rs, pbase + (unsigned)(b * 128 + gw * 32 + c * 2) * 8u, 8 * 1024, tag, r, d0, d1);
  }
  tile_store(lds + L_T + (grpA ? 2 : 3) * TILE, gw, r, c, tb);
  lds_arrive(cnt + (grpA ? C_T2 : C_T3), lane);
  if (hw == 4 && mlp) {
    const unsigned b1p = *reinterpret_cast<const unsigned*>(p.b1 + g * 128 + lane * 2);
    __builtin_amdgcn_s_sleep(64);
    sweep8<64, true>(rs, pbase + (unsigned)b * (8 * 1024) + (unsigned)lane * 16u, 2048, 1024, tag, 0xffu, gr);
    float t0 = 0.f, t1 = 0.f;
#pragma unroll
    for (int j = 0; j < 8; ++j) { t0 += __uint_as_float(gr[2 * j]); t1 += __uint_as_float(gr[2 * j + 1]); }
    t0 = bfr(bfr(t0) + bf_lo(b1p)); t1 = bfr(bfr(t1) + bf_hi(b1p));
    const float e0 = 0.5f * t0 * (1.f + tanhf(0.7978845608028654f * (t0 + 0.044715f * t0 * t0 * t0)));
    const float e1 = 0.5f * t1 * (1.f + tanhf(0.7978845608028654f * (t1 + 0.044715f * t1 * t1 * t1)));
    put_granule((gu64*)(p.ws + WS_HG) + (size_t)(g * NB + b) * 64 + lane, tag, pack_bf16(e0, e1));
    STAMP(4);
  }
  lds_wait<2>(cnt + (grpA ? C_T2 : C_T3), 4);
  tile_frags(lds + L_T + (grpA ? 2 : 3) * TILE, gw, r, c, raw);
  pair_tile(raw, bf);
#pragma unroll
  for (int k = 0; k < 8; ++k) asm volatile("" : "+v"(bf[k]));
  d0 = f32x4{0.f, 0.f, 0.f, 0.f}; d1 = f32x4{0.f, 0.f, 0.f, 0.f};
  f32x4* dx = reinterpret_cast<f32x4*>(lds + L_D + (gw * 64 + lane) * 8);
  if (grpA) {
    if (mlp) {
      if (hw == 0) {
        __builtin_amdgcn_s_sleep(127); __builtin_amdgcn_s_sleep(127);
        sweep8<127, true>(rs, (unsigned)WS_HG + (unsigned)g * 4096u + (unsigned)lane * 16u, 2048, 1024, tag, 0x0fu, gr);
#pragma unroll
        for (int j = 0; j < 4; ++j) *reinterpret_cast<u32x2*>(lds + L_H + j * 128 + lane * 2) = u32x2{gr[2 * j], gr[2 * j + 1]};
        lds_arrive(cnt + C_H, lane);
      }
      lds_wait<32>(cnt + C_H, 1);
      act_frags(lds + L_H, r, c, af);
      mfma_tile(af, bf, d0, d1);
    }
    dx[0] = d0; dx[1] = d1;
    lds_arrive(cnt + C_D, lane);
    STAMP(32 + hw);
    if (FB && hw == 1) {
      unsigned prev = 0u, ptk = 0u;
      for (int k = 0; k < 256; ++k) {
        unsigned tk = (unsigned)__builtin_amdgcn_s_memrealtime() >> FB_SH;
        const unsigned tk0 = tk;
        for (int n = 0; n < 4096 && tk == tk0; ++n) {
          __builtin_amdgcn_s_sleep(2);
          tk = (unsigned)__builtin_amdgcn_s_memrealtime() >> FB_SH;
        }
        const unsigned own = __hip_atomic_load(cnt + C_PROG, __ATOMIC_RELAXED, __HIP_MEMORY_SCOPE_WORKGROUP);
        if (own >= (unsigned)(NSW * (NCH - 3 - XBE))) break;
        __builtin_amdgcn_raw_buffer_store_b32((((tag << 4) | (tk & 15u)) << 10) | own, rs,
                                              lane < 32 ? (unsigned)WS_PR + ((tk & 1u) * 256u + bid) * 128u + lane * 4u : OOB, 0, 16);
        const unsigned e = __builtin_amdgcn_raw_buffer_load_b32(
            rs, lane < FB_N ? (unsigned)WS_PR + ((~tk & 1u) * 256u + ((idx + tk * 5u + xcd * 4u + (lane >> 3) * 16u) & 31u) * 8u + (lane & 7)) * 128u : OOB, 0, 16);
        const bool ok = lane < FB_N && (e >> 10) == (((tag << 4) | ((tk - 1u) & 15u)) & 0x3fffffu);
        const int nv = __popcll(__ballot(ok));
        const float sum = wave_sum(ok ? (float)(e & 1023u) : 0.f);
        int lv = -1;
        if (nv > FB_N / 2 && k > 0 && ptk + 1u == tk) {
          const float d = (float)prev - sum / (float)nv;
          lv = min(max(lv0 + (int)(d * (FB_NUM * 0.25f)), 0), FB_MAX);
        } else if (own > (unsigned)(NSW * NCH / 2)) {
          lv = 0;
        }
        if (lane == 0 && lv >= 0) __hip_atomic_store(cnt + C_LV, (unsigned)lv, __ATOMIC_RELAXED, __HIP_MEMORY_SCOPE_WORKGROUP);
        prev = own; ptk = tk;
      }
    }
    if (hw == 0) {
      const int ob = bid & 7, on = (bid >> 3) * 32 + c * 2;
      const unsigned xv = *reinterpret_cast<const unsigned*>(p.x + ob * DM + on);
      const unsigned bv = *reinterpret_cast<const unsigned*>(p.b2 + on);
      lds_wait<4>(cnt + C_O, 1);
      sweep8<PSLEEP, true>(rs, (unsigned)(WS_PG + ((size_t)((ob * 32 + (bid >> 3)) * NH + r * 8) * 32 + c * 2) * 8), 512, 256, tag, 0xffu, gr);
      float t0 = 0.f, t1 = 0.f;
#pragma unroll
      for (int j = 0; j < 8; ++j) { t0 += __uint_as_float(gr[2 * j]); t1 += __uint_as_float(gr[2 * j + 1]); }
      t0 = xrow_sum(t0) + (bf_lo(xv) + bf_lo(bv));
      t1 = xrow_sum(t1) + (bf_hi(xv) + bf_hi(bv));
      if (r == 0) *reinterpret_cast<unsigned*>(p.out + ob * DM + on) = pack_bf16(t0, t1);
      if (bid == 0 && lane == 0) __hip_atomic_store((gu32*)(p.ws + WS_G), tag, RLX_AGENT);
      STAMP(5);
    }
  } else {
    lds_wait<32>(cnt + C_D, 4);
    d0 = dx[0]; d1 = dx[1];
    if (hw == 4) {
      lds_wait<4>(cnt + C_STREAM, NSW);
      sweep8<PSLEEP, true>(rs, (unsigned)WS_OG + (unsigned)h * 4096u + (unsigned)lane * 16u, 2048, 1024, tag, 0x0fu, gr);
#pragma unroll
      for (int j = 0; j < 4; ++j) *reinterpret_cast<u32x2*>(lds + L_H + j * 128 + lane * 2) = u32x2{gr[2 * j], gr[2 * j + 1]};
      lds_arrive(cnt + C_O, lane);
    } else {
      lds_wait<8>(cnt + C_STREAM, NSW);
    }
    lds_wait<1>(cnt + C_O, 1);
    act_frags(lds + L_H, r, c, af);
    mfma_tile(af, bf, d0, d1);
    put_rows(rs, (unsigned)WS_PG + (unsigned)(((b * 4 + gw) * NH + h) * 32 + c * 2) * 8u, 32 * NH * 32 * 8, tag, r, d0, d1);
    STAMP(40 + gw);
  }
}

}

extern "C" void kernel_launch(void* const* d_in, const int* in_sizes, int n_in, void* d_out, int out_size, void* d_ws,
                              size_t ws_size, hipStream_t stream) {
  Params p;
  p.x = (const unsigned short*)d_in[0];
  p.kc = (const unsigned short*)d_in[1];
  p.vc = (const unsigned short*)d_in[2];
  p.wq = (const unsigned short*)d_in[3];
  p.wo = (const unsigned short*)d_in[4];
  p.w1 = (const unsigned short*)d_in[6];
  p.b1 = (const unsigned short*)d_in[7];
  p.w2 = (const unsigned short*)d_in[8];
  p.b2 = (const unsigned short*)d_in[9];
  p.out = (unsigned short*)d_out;
  p.ws = (unsigned char*)d_ws;
  fused_decode_kernel<<<dim3(NB * NH), dim3(NT), 0, stream>>>(p);
}
```

```cpp
#include <hip/hip_runtime.h>
#include <hip/hip_bf16.h>
#include <stdint.h>

#define MIMIC 1
#define USE_MEMSET 0

namespace {

constexpr int NB = 8, DM = 1024, NH = 32, SK = 4096, DH = 128, FF = 3584, HD = NH * DH;
#define CFG_NSW 8
#define CFG_NL 2
#define CFG_XB 3
#define CFG_DELAY 0
#define CFG_ROT 0
#define PACE_END 0
#define PACE_BEG 400
#define CFG_EB 2
#define CFG_EBS 0
#define XM 0
#define FB 1
#define FB_BASE 4
#define FB_MAX 16
#define FB_NUM 4
#define FB_N 16
#define FB_SH 10
#define MSH 1
#define CFG_WIN 1
#define TWIN 2
#define PSLEEP 4
#define KAUX 2
#define VAUX 2
#define WAUX 2
constexpr int NSW = CFG_NSW, NHW = 8, NT = (NSW + NHW) * 64;
constexpr int NL = CFG_NL;
constexpr int CHK = NL * 4;
constexpr int NCH = SK / CHK / NSW;
constexpr int XBE = CFG_XB + CFG_EB + (CFG_EB ? (CFG_EBS ? CFG_EB : 1) : 0);
constexpr unsigned SPIN_MAX = 200000u;

constexpr int NG = FF / 128;
constexpr size_t WS_G = 0;
constexpr size_t WS_OG = 4096;
constexpr size_t WS_HG = WS_OG + (size_t)NH * NB * 64 * 8;
constexpr size_t WS_QG = WS_HG + (size_t)NG * NB * 64 * 8;
constexpr size_t WS_UG = WS_QG + (size_t)NH * NB * NB * 128 * 8;
constexpr size_t WS_PG = WS_UG + (size_t)NG * NB * NB * 128 * 8;
constexpr size_t WS_PR = WS_PG + (size_t)NB * DM * NH * 8;
constexpr size_t WS_END = WS_PR + 2 * 256 * 128;

typedef __attribute__((ext_vector_type(2))) __bf16 bf16x2;
typedef __attribute__((ext_vector_type(2))) float f32x2;
typedef __attribute__((ext_vector_type(4))) float f32x4;
typedef __attribute__((ext_vector_type(4))) unsigned u32x4;
typedef __attribute__((ext_vector_type(2))) unsigned u32x2;
typedef __attribute__((ext_vector_type(8))) __bf16 bf16x8;
typedef __attribute__((address_space(1))) unsigned gu32;
typedef __attribute__((address_space(1))) unsigned long long gu64;
#define RLX_AGENT __ATOMIC_RELAXED, __HIP_MEMORY_SCOPE_AGENT

__device__ __forceinline__ float dot2(unsigned a, unsigned b, float acc) {
  return __builtin_amdgcn_fdot2_f32_bf16(__builtin_bit_cast(bf16x2, a), __builtin_bit_cast(bf16x2, b), acc, false);
}
__device__ __forceinline__ unsigned pack_bf16(float lo, float hi) {
  f32x2 v = {lo, hi};
  return __builtin_bit_cast(unsigned, __builtin_convertvector(v, bf16x2));
}
__device__ __forceinline__ float bf_lo(unsigned u) { return __builtin_bit_cast(float, u << 16); }
__device__ __forceinline__ float bf_hi(unsigned u) { return __builtin_bit_cast(float, u & 0xffff0000u); }
__device__ __forceinline__ float bfr(float x) { return bf_lo(pack_bf16(x, 0.f)); }
template <int CTRL> __device__ __forceinline__ float dpp(float x) {
  return __builtin_bit_cast(float, __builtin_amdgcn_mov_dpp(__builtin_bit_cast(int, x), CTRL, 0xf, 0xf, true));
}
constexpr int XOR1 = 0xB1, XOR2 = 0x4E, XOR7 = 0x141, XOR8 = 0x128;
__device__ __forceinline__ float row_sum(float x) {
  x += dpp<XOR1>(x); x += dpp<XOR2>(x); x += dpp<XOR7>(x); x += dpp<XOR8>(x); return x;
}
__device__ __forceinline__ float xrow_sum(float x) {
  auto s = __builtin_amdgcn_permlane16_swap(__float_as_uint(x), __float_as_uint(x), false, false);
  x = __uint_as_float(s[0]) + __uint_as_float(s[1]);
  auto t = __builtin_amdgcn_permlane32_swap(__float_as_uint(x), __float_as_uint(x), false, false);
  return __uint_as_float(t[0]) + __uint_as_float(t[1]);
}
__device__ __forceinline__ float xrow_max(float x) {
  auto s = __builtin_amdgcn_permlane16_swap(__float_as_uint(x), __float_as_uint(x), false, false);
  x = fmaxf(__uint_as_float(s[0]), __uint_as_float(s[1]));
  auto t = __builtin_amdgcn_permlane32_swap(__float_as_uint(x), __float_as_uint(x), false, false);
  return fmaxf(__uint_as_float(t[0]), __uint_as_float(t[1]));
}
__device__ __forceinline__ float wave_sum(float x) { return xrow_sum(row_sum(x)); }
__device__ __forceinline__ unsigned perm_lo(unsigned hi_src, unsigned lo_src) { return __builtin_amdgcn_perm(hi_src, lo_src, 0x05040100u); }
__device__ __forceinline__ unsigned perm_hi(unsigned hi_src, unsigned lo_src) { return __builtin_amdgcn_perm(hi_src, lo_src, 0x07060302u); }
__device__ __forceinline__ u32x4 ld16(const void* p) { return *reinterpret_cast<const u32x4*>(p); }
__device__ __forceinline__ u32x4 ld16nt(const void* p) { return __builtin_nontemporal_load(reinterpret_cast<const u32x4*>(p)); }

template <int AUX>
__device__ __forceinline__ u32x4 ldkv(__amdgpu_buffer_rsrc_t rs, unsigned vo, unsigned so, int i) {
  return __builtin_amdgcn_raw_buffer_load_b128(rs, vo + (i & 3) * 1024, so + (i >> 2) * 4096, AUX);
}

struct LnStat { float sub, den; };
__device__ __forceinline__ LnStat ln_stat(const float (&v)[16]) {
  float s = 0.f;
#pragma unroll
  for (int i = 0; i < 16; ++i) s += v[i];
  const float mean = wave_sum(s) * (1.f / DM);
  float q = 0.f;
#pragma unroll
  for (int i = 0; i < 16; ++i) { const float d = v[i] - mean; q = fmaf(d, d, q); }
  const float var = wave_sum(q) * (1.f / DM);
  LnStat st;
  st.sub = bfr(mean);
  st.den = bfr(sqrtf(bfr(bfr(var) + 1e-5f)));
  return st;
}
__device__ __forceinline__ float ln_apply(float x, LnStat st) {
  return bfr(x - st.sub) / st.den;
}
__device__ __forceinline__ void unpack16(u32x4 a, u32x4 b, float (&v)[16]) {
#pragma unroll
  for (int j = 0; j < 4; ++j) { v[2 * j] = bf_lo(a[j]); v[2 * j + 1] = bf_hi(a[j]); v[8 + 2 * j] = bf_lo(b[j]); v[8 + 2 * j + 1] = bf_hi(b[j]); }
}
#define STAMP(i) do {} while (0)

struct Params {
  const unsigned short* x; const unsigned short* kc; const unsigned short* vc; const unsigned short* wq; const unsigned short* wo;
  const unsigned short* w1; const unsigned short* b1; const unsigned short* w2; const unsigned short* b2;
  unsigned short* out; unsigned char* ws;
};

constexpr int TROW = 68;
constexpr int TILE = 128 * TROW;
constexpr int L_T = 0;
constexpr int L_Q = L_T + 4 * TILE;
constexpr int L_XN = L_Q + 64;
constexpr int L_Y = L_XN + 512;
constexpr int L_H = L_Y + 512;
constexpr int L_D = L_H + 512;
constexpr int L_REC = L_D + 2048;
constexpr int REC = 132;
constexpr int L_CNT = L_REC + NSW * REC;
constexpr int L_END = L_CNT + 16;
enum { C_STREAM = 0, C_XN, C_Y, C_TAG, C_Q, C_T0, C_T1, C_T2, C_T3, C_H, C_D, C_O, C_LV, C_PROG };
constexpr unsigned OOB = 0xfffffff0u;

template <int N> __device__ __forceinline__ void window() {
  __builtin_amdgcn_sched_barrier(0);
  asm volatile("s_waitcnt vmcnt(%0)" ::"i"(N) : "memory");
  __builtin_amdgcn_sched_barrier(0);
}
template <int ROWB, bool WIN>
__device__ __forceinline__ void tile_load(__amdgpu_buffer_rsrc_t rw, unsigned off, u32x4 (&t)[8]) {
#pragma unroll
  for (int i = 0; i < 8; ++i) {
    if (WIN && (i & 1) == 0) window<TWIN>();
    t[i] = __builtin_amdgcn_raw_buffer_load_b128(rw, off, i * 4 * ROWB, WAUX);
  }
}
__device__ __forceinline__ void tile_store(float* tile, int gw, int r, int c, const u32x4 (&t)[8]) {
#pragma unroll
  for (int i = 0; i < 8; ++i) *reinterpret_cast<u32x4*>(tile + (gw * 32 + i * 4 + r) * TROW + c * 4) = t[i];
}
__device__ __forceinline__ void tile_frags(const float* tile, int gw, int r, int c, unsigned (&raw)[32]) {
#pragma unroll
  for (int i = 0; i < 32; ++i) raw[i] = __float_as_uint(tile[((i >> 3) * 32 + r * 8 + (i & 7)) * TROW + gw * 16 + c]);
}
__device__ __forceinline__ void act_frags(const float* act, int r, int c, u32x4 (&af)[4]) {
#pragma unroll
  for (int kk = 0; kk < 4; ++kk) {
    af[kk] = *reinterpret_cast<const u32x4*>(act + (c & 7) * 64 + kk * 16 + r * 4);
    if (c >= 8) af[kk] = u32x4{0u, 0u, 0u, 0u};
  }
}
__device__ __forceinline__ void pair_tile(const unsigned (&raw)[32], u32x4 (&bf)[8]) {
#pragma unroll
  for (int kk = 0; kk < 4; ++kk)
#pragma unroll
    for (int e = 0; e < 4; ++e) {
      bf[kk * 2][e] = perm_lo(raw[kk * 8 + 2 * e + 1], raw[kk * 8 + 2 * e]);
      bf[kk * 2 + 1][e] = perm_hi(raw[kk * 8 + 2 * e + 1], raw[kk * 8 + 2 * e]);
    }
}
__device__ __forceinline__ void mfma_tile(const u32x4 (&af)[4], const u32x4 (&bf)[8], f32x4& d0, f32x4& d1) {
#pragma unroll
  for (int kk = 0; kk < 4; ++kk) {
    d0 = __builtin_amdgcn_mfma_f32_16x16x32_bf16(__builtin_bit_cast(bf16x8, af[kk]), __builtin_bit_cast(bf16x8, bf[kk * 2]), d0, 0, 0, 0);
    d1 = __builtin_amdgcn_mfma_f32_16x16x32_bf16(__builtin_bit_cast(bf16x8, af[kk]), __builtin_bit_cast(bf16x8, bf[kk * 2 + 1]), d1, 0, 0, 0);
  }
}
template <int SLEEP, bool NARROW>
__device__ __forceinline__ void sweep8(__amdgpu_buffer_rsrc_t rs, unsigned off0, unsigned shi, unsigned slo, unsigned tag,
                                       unsigned pend, unsigned (&v)[16]) {
#pragma unroll
  for (int j = 0; j < 16; ++j) v[j] = 0u;
  unsigned act = pend;
  for (unsigned spins = 0; spins < SPIN_MAX; ++spins) {
    u32x4 g[8];
#pragma unroll
    for (int j = 0; j < 8; ++j) {
      const unsigned off = ((act >> j) & 1u) ? off0 + (j >> 1) * shi + (j & 1) * slo : OOB;
      g[j] = __builtin_amdgcn_raw_buffer_load_b128(rs, off, 0, 16);
    }
    __builtin_amdgcn_sched_barrier(0);
    const unsigned before = pend;
#pragma unroll
    for (int j = 0; j < 8; ++j) {
      const bool ok = ((act >> j) & 1u) && g[j][1] == tag && g[j][3] == tag;
      v[2 * j] = ok ? g[j][0] : v[2 * j];
      v[2 * j + 1] = ok ? g[j][2] : v[2 * j + 1];
      pend = ok ? (pend & ~(1u << j)) : pend;
    }
    if (!__any(pend != 0u)) break;
    if (NARROW) {
      const bool was_narrow_hit = __any(pend != before && act != before);
      act = was_narrow_hit ? pend : (pend & (0u - pend));
      if (!was_narrow_hit) __builtin_amdgcn_s_sleep(SLEEP);
    } else {
      act = pend;
      __builtin_amdgcn_s_sleep(SLEEP);
    }
  }
}
__device__ __forceinline__ void put_granule(gu64* g, unsigned tag, unsigned val) {
  __hip_atomic_store(g, ((unsigned long long)tag << 32) | val, RLX_AGENT);
}
__device__ __forceinline__ void put_rows(__amdgpu_buffer_rsrc_t rs, unsigned off, unsigned rstride, unsigned tag, int r,
                                         const f32x4& d0, const f32x4& d1) {
  if (r < 2) {
#pragma unroll
    for (int i = 0; i < 4; ++i)
      __builtin_amdgcn_raw_buffer_store_b128(u32x4{__float_as_uint(d0[i]), tag, __float_as_uint(d1[i]), tag}, rs,
                                             off + (r * 4 + i) * rstride, 0, 16);
  }
}
__device__ __forceinline__ void lds_arrive(unsigned* cnt, int lane) {
  asm volatile("s_waitcnt lgkmcnt(0)" ::: "memory");
  if (lane == 0) __hip_atomic_fetch_add(cnt, 1u, __ATOMIC_RELAXED, __HIP_MEMORY_SCOPE_WORKGROUP);
}
template <int SLEEP>
__device__ __forceinline__ void lds_wait(unsigned* cnt, unsigned n) {
  for (unsigned spins = 0; spins < 50u * SPIN_MAX; ++spins) {
    if (__hip_atomic_load(cnt, __ATOMIC_RELAXED, __HIP_MEMORY_SCOPE_WORKGROUP) >= n) break;
    __builtin_amdgcn_s_sleep(SLEEP);
  }
  asm volatile("" ::: "memory");
}

__device__ __forceinline__ void hold(int lv) {
  __builtin_amdgcn_sched_barrier(0);
#pragma nounroll
  for (int n = lv; n > 0; --n) __builtin_amdgcn_s_sleep(1);
  __builtin_amdgcn_sched_barrier(0);
}
template <bool PF>
__device__ __forceinline__ void attn_step(u32x4 (&Kb)[NL], u32x4 (&Vb)[NL], __amdgpu_buffer_rsrc_t rsk, __amdgpu_buffer_rsrc_t rsv, unsigned so, unsigned vo,
                                          const u32x4& q, float& m, float& l, float (&acc)[8], int lv = 0) {
  float s[NL];
#pragma unroll
  for (int i = 0; i < NL; ++i) {
    float d = dot2(Kb[i][0], q[0], 0.f);
    d = dot2(Kb[i][1], q[1], d); d = dot2(Kb[i][2], q[2], d); d = dot2(Kb[i][3], q[3], d);
    s[i] = d;
  }
  if (PF) {
#pragma unroll
    for (int i = 0; i < NL; ++i) asm volatile("" : "+v"(s[i]));
    hold(lv);
#pragma unroll
    for (int i = 0; i < NL; ++i) Kb[i] = ldkv<KAUX>(rsk, vo, so, i);
  }
  __builtin_amdgcn_sched_barrier(0);
  constexpr float SCALE = 0.08838834764831845f, LOG2E = 1.4426950408889634f;
#pragma unroll
  for (int i = 0; i < NL; ++i) s[i] = row_sum(s[i]);
#pragma unroll
  for (int i = 0; i < NL; i += 2) {
    unsigned u = pack_bf16(s[i], s[i + 1]);
    u = pack_bf16(bf_lo(u) * SCALE, bf_hi(u) * SCALE);
    s[i] = bf_lo(u) * LOG2E; s[i + 1] = bf_hi(u) * LOG2E;
  }
  float mn = m;
#pragma unroll
  for (int i = 0; i < NL; ++i) mn = fmaxf(mn, s[i]);
  const float alpha = __builtin_amdgcn_exp2f(m - mn);
  m = mn;
  float ps = 0.f;
#pragma unroll
  for (int i = 0; i < NL; ++i) { s[i] = __builtin_amdgcn_exp2f(s[i] - mn); ps += s[i]; }
  l = fmaf(l, alpha, ps);
#pragma unroll
  for (int k = 0; k < 8; ++k) acc[k] *= alpha;
#pragma unroll
  for (int i = 0; i < NL; ++i) {
#pragma unroll
    for (int j = 0; j < 4; ++j) {
      acc[2 * j] = fmaf(s[i], bf_lo(Vb[i][j]), acc[2 * j]);
      acc[2 * j + 1] = fmaf(s[i], bf_hi(Vb[i][j]), acc[2 * j + 1]);
    }
  }
  if (PF) {
#pragma unroll
    for (int k = 0; k < 8; ++k) asm volatile("" : "+v"(acc[k]));
    hold(lv);
#pragma unroll
    for (int i = 0; i < NL; ++i) Vb[i] = ldkv<VAUX>(rsv, vo, so, i);
  }
  __builtin_amdgcn_sched_barrier(0);
}

__device__ __forceinline__ void q_phase(const Params& p, float* lds, __amdgpu_buffer_rsrc_t rs, int h, int b, int hw, int lane) {
  unsigned* cnt = reinterpret_cast<unsigned*>(lds) + L_CNT;
  const int r = lane >> 4, c = lane & 15;
  u32x4 bf[8], af[4];
  unsigned raw[32];
  f32x4 d0 = {0.f, 0.f, 0.f, 0.f}, d1 = {0.f, 0.f, 0.f, 0.f};
  lds_wait<1>(cnt + C_T0, 4);
  tile_frags(lds + L_T, hw, r, c, raw);
  pair_tile(raw, bf);
  lds_wait<1>(cnt + C_XN, NHW);
  const unsigned tag = cnt[C_TAG];
  act_frags(lds + L_XN, r, c, af);
  mfma_tile(af, bf, d0, d1);
  const unsigned pbase = (unsigned)WS_QG + (unsigned)h * (64 * 1024);
  put_rows(rs, pbase + (unsigned)(b * 128 + hw * 32 + c * 2) * 8u, 8 * 1024, tag, r, d0, d1);
  if (hw == 0) {
    unsigned gr[16];
    sweep8<1, false>(rs, pbase + (unsigned)b * (8 * 1024) + (unsigned)lane * 16u, 2048, 1024, tag, 0xffu, gr);
    float t0 = 0.f, t1 = 0.f;
#pragma unroll
    for (int j = 0; j < 8; ++j) { t0 += __uint_as_float(gr[2 * j]); t1 += __uint_as_float(gr[2 * j + 1]); }
    reinterpret_cast<unsigned*>(lds)[L_Q + lane] = pack_bf16(t0, t1);
    lds_arrive(cnt + C_Q, lane);
  }
}

__global__ void __launch_bounds__(NT, NT / 256) fused_decode_kernel(Params p) {
  __shared__ __attribute__((aligned(16))) float lds[L_END];
  unsigned* ldsu = reinterpret_cast<unsigned*>(lds);
  const int bid = blockIdx.x, xcd = bid & 7, idx = bid >> 3;
  const int h = xcd * 4 + (idx >> 3), b = idx & 7;
  const int tid = threadIdx.x, lane = tid & 63, wave = __builtin_amdgcn_readfirstlane(tid >> 6);
  const int r = lane >> 4, c = lane & 15;
  const bool streamer = wave >= NHW;
  if (wave == 0) STAMP(0);

  if (streamer) {
    const int sw = wave - NHW;
    const size_t slab = (size_t)(b * NH + h) * SK * DH;
    const __amdgpu_buffer_rsrc_t rsk = __builtin_amdgcn_make_buffer_rsrc((void*)(p.kc + slab), 0, SK * DH * 2, 0x00020000);
    const __amdgpu_buffer_rsrc_t rsv = __builtin_amdgcn_make_buffer_rsrc((void*)(p.vc + slab), 0, SK * DH * 2, 0x00020000);
    unsigned so = (unsigned)sw * (CHK * DH * 2);
    const unsigned vo = (unsigned)lane * 16u;
    constexpr int STRIDE = NSW * CHK * DH * 2;
    constexpr int XB = CFG_XB;
    u32x4 Kb[NL], Vb[NL], Kx[XB > 0 ? XB : 1][NL], Vx[XB > 0 ? XB : 1][NL];
    __syncthreads();
    if (CFG_DELAY) __builtin_amdgcn_s_sleep(CFG_DELAY);
#pragma unroll
    for (int i = 0; i < NL; ++i) Kb[i] = ldkv<KAUX>(rsk, vo, so, i);
#pragma unroll
    for (int i = 0; i < NL; ++i) Vb[i] = ldkv<VAUX>(rsv, vo, so, i);
#pragma unroll
    for (int x = 0; x < XB; ++x) {
      window<NL>();
#pragma unroll
      for (int i = 0; i < NL; ++i) Kx[x][i] = ldkv<KAUX>(rsk, vo, so + (NCH - 1 - x) * STRIDE, i);
      window<NL>();
#pragma unroll
      for (int i = 0; i < NL; ++i) Vx[x][i] = ldkv<VAUX>(rsv, vo, so + (NCH - 1 - x) * STRIDE, i);
    }
    __builtin_amdgcn_sched_barrier(0);
    lds_wait<2>(ldsu + L_CNT + C_Q, 1);
    if (sw == 0) STAMP(2);
    const unsigned tag = ldsu[L_CNT + C_TAG];
    const u32x4 q = *reinterpret_cast<const u32x4*>(lds + L_Q + c * 4);
    float m = -INFINITY, l = 0.f, acc[8];
#pragma unroll
    for (int k = 0; k < 8; ++k) acc[k] = 0.f;
    if (XB > 0) {
      so += STRIDE;
      attn_step<true>(Kb, Vb, rsk, rsv, so, vo, q, m, l, acc);
#pragma unroll
      for (int x = 0; x < XB; ++x) attn_step<false>(Kx[x], Vx[x], rsk, rsv, so, vo, q, m, l, acc);
    }
    constexpr int EB = CFG_EB;
    const unsigned eo = (unsigned)sw * (CHK * DH * 2) + (NCH - 1 - XB) * STRIDE;
    for (int it = 1; it < NCH - 1 - XB - EB - (EB ? (CFG_EBS ? EB : 1) : 0); ++it) {
      so += STRIDE;
      const int lv = __builtin_amdgcn_readfirstlane(__hip_atomic_load(ldsu + L_CNT + C_LV, __ATOMIC_RELAXED, __HIP_MEMORY_SCOPE_WORKGROUP));
      if (FB && lane == 0) __hip_atomic_fetch_add(ldsu + L_CNT + C_PROG, 1u, __ATOMIC_RELAXED, __HIP_MEMORY_SCOPE_WORKGROUP);
      attn_step<true>(Kb, Vb, rsk, rsv, so, vo, q, m, l, acc, lv);
    }
    if (EB) {
#pragma unroll
      for (int x = 0; x < EB; ++x) {
#pragma unroll
        for (int i = 0; i < NL; ++i) Kx[x][i] = ldkv<KAUX>(rsk, vo, eo - x * STRIDE, i);
#pragma unroll
        for (int i = 0; i < NL; ++i) Vx[x][i] = ldkv<VAUX>(rsv, vo, eo - x * STRIDE, i);
        __builtin_amdgcn_sched_barrier(0);
        if (CFG_EBS || x == EB - 1) {
          so += STRIDE;
          attn_step<true>(Kb, Vb, rsk, rsv, so, vo, q, m, l, acc);
        }
      }
#pragma unroll
      for (int x = 0; x < EB; ++x) attn_step<false>(Kx[x], Vx[x], rsk, rsv, so, vo, q, m, l, acc);
    }
    attn_step<false>(Kb, Vb, rsk, rsv, so, vo, q, m, l, acc);
    STAMP(16 + sw);
    const float M = xrow_max(m);
    const float wt = __builtin_amdgcn_exp2f(m - M);
    l = xrow_sum(l * wt);
#pragma unroll
    for (int k = 0; k < 8; ++k) acc[k] = xrow_sum(acc[k] * wt);
    float* rec = lds + L_REC + sw * REC;
    if (r == 0) {
      f32x4* d = reinterpret_cast<f32x4*>(rec + 4 + c * 8);
      d[0] = f32x4{acc[0], acc[1], acc[2], acc[3]}; d[1] = f32x4{acc[4], acc[5], acc[6], acc[7]};
      if (c == 0) { rec[0] = M; rec[1] = l; }
    }
    unsigned arrived = 0;
    if (lane == 0) arrived = __hip_atomic_fetch_add(ldsu + L_CNT + C_STREAM, 1u, __ATOMIC_ACQ_REL, __HIP_MEMORY_SCOPE_WORKGROUP);
    arrived = __builtin_amdgcn_readfirstlane(arrived);
    if (arrived == NSW - 1) {
      __builtin_amdgcn_fence(__ATOMIC_ACQUIRE, "workgroup");
      float MM = -INFINITY;
#pragma unroll
      for (int w = 0; w < NSW; ++w) MM = fmaxf(MM, lds[L_REC + w * REC]);
      float L = 0.f, o0 = 0.f, o1 = 0.f;
#pragma unroll
      for (int w = 0; w < NSW; ++w) {
        const float* rw = lds + L_REC + w * REC;
        const float e = __builtin_amdgcn_exp2f(rw[0] - MM);
        const f32x2 a = *reinterpret_cast<const f32x2*>(rw + 4 + 2 * lane);
        L = fmaf(e, rw[1], L); o0 = fmaf(e, a[0], o0); o1 = fmaf(e, a[1], o1);
      }
      const float inv = 1.f / L;
      const unsigned pay = pack_bf16(o0 * inv, o1 * inv);
      gu64* og = (gu64*)(p.ws + WS_OG) + (size_t)(h * NB + b) * 64 + lane;
      __hip_atomic_store(og, ((unsigned long long)tag << 32) | pay, RLX_AGENT);
      STAMP(3);
    }
    return;
  }

  const int hw = wave, gw = wave & 3;
  const bool grpA = hw < 4;
  __builtin_amdgcn_s_setprio(3);
  unsigned* cnt = ldsu + L_CNT;
  const __amdgpu_buffer_rsrc_t rs = __builtin_amdgcn_make_buffer_rsrc(p.ws, 0, (int)WS_END, 0x00020000);
  const bool mlp = idx < 24 || (xcd & 1) == 0;
  const int g = idx < 24 ? xcd * 3 + (idx >> 3) : 24 + (xcd >> 1);

  const unsigned short* xr = p.x + hw * DM + lane * 16;
  const u32x4 x0 = ld16(xr), x1 = ld16(xr + 8);
  unsigned g0 = 0;
  g0 = __hip_atomic_load((gu32*)(p.ws + WS_G), RLX_AGENT);
  u32x4 ta[8], tb[8];
  if (grpA) {
    const __amdgpu_buffer_rsrc_t r0 = __builtin_amdgcn_make_buffer_rsrc((void*)p.wq, 0, DM * HD * 2, 0x00020000);
    tile_load<HD * 2, false>(r0, (unsigned)((b * 128 + gw * 32 + r) * HD + h * 128 + c * 8) * 2u, ta);
  }
  const int lv0 = (xcd & 1) ? 0 : FB_BASE;
  if (tid < 16) cnt[tid] = tid == C_LV ? lv0 : 0u;
  __builtin_amdgcn_sched_barrier(0);
  __syncthreads();
  {
    float v[16];
    unpack16(x0, x1, v);
    const LnStat s1 = ln_stat(v);
    u32x4 y0, y1;
#pragma unroll
    for (int j = 0; j < 4; ++j) {
      y0[j] = pack_bf16(ln_apply(v[2 * j], s1), ln_apply(v[2 * j + 1], s1));
      y1[j] = pack_bf16(ln_apply(v[8 + 2 * j], s1), ln_apply(v[8 + 2 * j + 1], s1));
    }
    if ((lane >> 3) == b) {
      u32x4* xd = reinterpret_cast<u32x4*>(lds + L_XN + hw * 64 + (lane & 7) * 8);
      xd[0] = y0; xd[1] = y1;
    }
    if (tid == 0) cnt[C_TAG] = g0 + 1u;
    lds_arrive(cnt + C_XN, lane);
    if (grpA) {
      tile_store(lds + L_T, gw, r, c, ta);
      lds_arrive(cnt + C_T0, lane);
      q_phase(p, lds, rs, h, b, hw, lane);
      const __amdgpu_buffer_rsrc_t r1 = __builtin_amdgcn_make_buffer_rsrc((void*)p.w2, 0, mlp ? FF * DM * 2 : 0, 0x00020000);
      tile_load<DM * 2, true>(r1, (unsigned)((g * 128 + gw * 32 + r) * DM + b * 128 + c * 8) * 2u, tb);
    }
    __builtin_amdgcn_sched_barrier(0);
    unpack16(y0, y1, v);
    const LnStat s2 = ln_stat(v);
#pragma unroll
    for (int j = 0; j < 4; ++j) {
      y0[j] = pack_bf16(ln_apply(v[2 * j], s2), ln_apply(v[2 * j + 1], s2));
      y1[j] = pack_bf16(ln_apply(v[8 + 2 * j], s2), ln_apply(v[8 + 2 * j + 1], s2));
    }
    if ((lane >> 3) == b) {
      u32x4* yd = reinterpret_cast<u32x4*>(lds + L_Y + hw * 64 + (lane & 7) * 8);
      yd[0] = y0; yd[1] = y1;
    }
    lds_arrive(cnt + C_Y, lane);
    if (!grpA) {
      const __amdgpu_buffer_rsrc_t r0 = __builtin_amdgcn_make_buffer_rsrc((void*)p.w1, 0, mlp ? DM * FF * 2 : 0, 0x00020000);
      tile_load<FF * 2, true>(r0, (unsigned)((b * 128 + gw * 32 + r) * FF + g * 128 + c * 8) * 2u, ta);
      const __amdgpu_buffer_rsrc_t r1 = __builtin_amdgcn_make_buffer_rsrc((void*)p.wo, 0, HD * DM * 2, 0x00020000);
      tile_load<DM * 2, true>(r1, (unsigned)((h * 128 + gw * 32 + r) * DM + b * 128 + c * 8) * 2u, tb);
      tile_store(lds + L_T + TILE, gw, r, c, ta);
      lds_arrive(cnt + C_T1, lane);
    }
  }
  u32x4 bf[8], af[4];
  unsigned raw[32], gr[16];
  f32x4 d0 = {0.f, 0.f, 0.f, 0.f}, d1 = {0.f, 0.f, 0.f, 0.f};
  lds_wait<1>(cnt + C_XN, NHW);
  const unsigned tag = cnt[C_TAG];
  const unsigned pbase = (unsigned)WS_UG + (unsigned)g * (64 * 1024);
  if (!grpA) {
    lds_wait<2>(cnt + C_T1, 4);
    tile_frags(lds + L_T + TILE, gw, r, c, raw);
    pair_tile(raw, bf);
    lds_wait<2>(cnt + C_Y, NHW);
    act_frags(lds + L_Y, r, c, af);
    mfma_tile(af, bf, d0, d1);
    if (mlp) put_rows(rs, pbase + (unsigned)(b * 128 + gw * 32 + c * 2) * 8u, 8 * 1024, tag, r, d0, d1);
  }
  tile_store(lds + L_T + (grpA ? 2 : 3) * TILE, gw, r, c, tb);
  lds_arrive(cnt + (grpA ? C_T2 : C_T3), lane);
  if (hw == 4 && mlp) {
    const unsigned b1p = *reinterpret_cast<const unsigned*>(p.b1 + g * 128 + lane * 2);
    __builtin_amdgcn_s_sleep(64);
    sweep8<64, true>(rs, pbase + (unsigned)b * (8 * 1024) + (unsigned)lane * 16u, 2048, 1024, tag, 0xffu, gr);
    float t0 = 0.f, t1 = 0.f;
#pragma unroll
    for (int j = 0; j < 8; ++j) { t0 += __uint_as_float(gr[2 * j]); t1 += __uint_as_float(gr[2 * j + 1]); }
    t0 = bfr(bfr(t0) + bf_lo(b1p)); t1 = bfr(bfr(t1) + bf_hi(b1p));
    const float e0 = 0.5f * t0 * (1.f + tanhf(0.7978845608028654f * (t0 + 0.044715f * t0 * t0 * t0)));
    const float e1 = 0.5f * t1 * (1.f + tanhf(0.7978845608028654f * (t1 + 0.044715f * t1 * t1 * t1)));
    put_granule((gu64*)(p.ws + WS_HG) + (size_t)(g * NB + b) * 64 + lane, tag, pack_bf16(e0, e1));
    STAMP(4);
  }
  lds_wait<2>(cnt + (grpA ? C_T2 : C_T3), 4);
  tile_frags(lds + L_T + (grpA ? 2 : 3) * TILE, gw, r, c, raw);
  pair_tile(raw, bf);
#pragma unroll
  for (int k = 0; k < 8; ++k) asm volatile("" : "+v"(bf[k]));
  d0 = f32x4{0.f, 0.f, 0.f, 0.f}; d1 = f32x4{0.f, 0.f, 0.f, 0.f};
  f32x4* dx = reinterpret_cast<f32x4*>(lds + L_D + (gw * 64 + lane) * 8);
  if (grpA) {
    if (mlp) {
      if (hw == 0) {
        __builtin_amdgcn_s_sleep(127); __builtin_amdgcn_s_sleep(127);
        sweep8<127, true>(rs, (unsigned)WS_HG + (unsigned)g * 4096u + (unsigned)lane * 16u, 2048, 1024, tag, 0x0fu, gr);
#pragma unroll
        for (int j = 0; j < 4; ++j) *reinterpret_cast<u32x2*>(lds + L_H + j * 128 + lane * 2) = u32x2{gr[2 * j], gr[2 * j + 1]};
        lds_arrive(cnt + C_H, lane);
      }
      lds_wait<32>(cnt + C_H, 1);
      act_frags(lds + L_H, r, c, af);
      mfma_tile(af, bf, d0, d1);
    }
    dx[0] = d0; dx[1] = d1;
    lds_arrive(cnt + C_D, lane);
    STAMP(32 + hw);
    if (FB && hw == 1) {
      unsigned prev = 0u, ptk = 0u;
      for (int k = 0; k < 256; ++k) {
        unsigned tk = (unsigned)__builtin_amdgcn_s_memrealtime() >> FB_SH;
        const unsigned tk0 = tk;
        constexpr unsigned DONE = NSW * (NCH - 3 - XBE);
        unsigned own = 0u;
        for (int n = 0; n < 4096 && tk == tk0 && own < DONE; ++n) {
          __builtin_amdgcn_s_sleep(2);
          tk = (unsigned)__builtin_amdgcn_s_memrealtime() >> FB_SH;
          own = __builtin_amdgcn_readfirstlane(__hip_atomic_load(cnt + C_PROG, __ATOMIC_RELAXED, __HIP_MEMORY_SCOPE_WORKGROUP));
        }
        if (own >= DONE) break;
        __builtin_amdgcn_raw_buffer_store_b32((((tag << 4) | (tk & 15u)) << 10) | own, rs,
                                              lane < 32 ? (unsigned)WS_PR + ((tk & 1u) * 256u + bid) * 128u + lane * 4u : OOB, 0, 16);
        const unsigned e = __builtin_amdgcn_raw_buffer_load_b32(
            rs, lane < FB_N ? (unsigned)WS_PR + ((~tk & 1u) * 256u + ((idx + tk * 5u + xcd * 4u + (lane >> 3) * 16u) & 31u) * 8u + (lane & 7)) * 128u : OOB, 0, 16);
        const bool ok = lane < FB_N && (e >> 10) == (((tag << 4) | ((tk - 1u) & 15u)) & 0x3fffffu);
        const int nv = __popcll(__ballot(ok));
        const float sum = wave_sum(ok ? (float)(e & 1023u) : 0.f);
        int lv = -1;
        if (nv > FB_N / 2 && k > 0 && ptk + 1u == tk) {
          const float d = (float)prev - sum / (float)nv;
          lv = min(max(lv0 + (int)(d * (FB_NUM * 0.25f)), 0), FB_MAX);
        } else if (own > (unsigned)(NSW * NCH / 2)) {
          lv = 0;
        }
        if (lane == 0 && lv >= 0) __hip_atomic_store(cnt + C_LV, (unsigned)lv, __ATOMIC_RELAXED, __HIP_MEMORY_SCOPE_WORKGROUP);
        prev = own; ptk = tk;
      }
    }
    if (hw == 0) {
      const int ob = bid & 7, on = (bid >> 3) * 32 + c * 2;
      const unsigned xv = *reinterpret_cast<const unsigned*>(p.x + ob * DM + on);
      const unsigned bv = *reinterpret_cast<const unsigned*>(p.b2 + on);
      lds_wait<4>(cnt + C_O, 1);
      sweep8<PSLEEP, true>(rs, (unsigned)(WS_PG + ((size_t)((ob * 32 + (bid >> 3)) * NH + r * 8) * 32 + c * 2) * 8), 512, 256, tag, 0xffu, gr);
      float t0 = 0.f, t1 = 0.f;
#pragma unroll
      for (int j = 0; j < 8; ++j) { t0 += __uint_as_float(gr[2 * j]); t1 += __uint_as_float(gr[2 * j + 1]); }
      t0 = xrow_sum(t0) + (bf_lo(xv) + bf_lo(bv));
      t1 = xrow_sum(t1) + (bf_hi(xv) + bf_hi(bv));
      if (r == 0) *reinterpret_cast<unsigned*>(p.out + ob * DM + on) = pack_bf16(t0, t1);
      if (bid == 0 && lane == 0) __hip_atomic_store((gu32*)(p.ws + WS_G), tag, RLX_AGENT);
      STAMP(5);
    }
  } else {
    lds_wait<32>(cnt + C_D, 4);
    d0 = dx[0]; d1 = dx[1];
    if (hw == 4) {
      lds_wait<4>(cnt + C_STREAM, NSW);
      sweep8<PSLEEP, true>(rs, (unsigned)WS_OG + (unsigned)h * 4096u + (unsigned)lane * 16u, 2048, 1024, tag, 0x0fu, gr);
#pragma unroll
      for (int j = 0; j < 4; ++j) *reinterpret_cast<u32x2*>(lds + L_H + j * 128 + lane * 2) = u32x2{gr[2 * j], gr[2 * j + 1]};
      lds_arrive(cnt + C_O, lane);
    } else {
      lds_wait<8>(cnt + C_STREAM, NSW);
    }
    lds_wait<1>(cnt + C_O, 1);
    act_frags(lds + L_H, r, c, af);
    mfma_tile(af, bf, d0, d1);
    put_rows(rs, (unsigned)WS_PG + (unsigned)(((b * 4 + gw) * NH + h) * 32 + c * 2) * 8u, 32 * NH * 32 * 8, tag, r, d0, d1);
    STAMP(40 + gw);
  }
}

}

extern "C" void kernel_launch(void* const* d_in, const int* in_sizes, int n_in, void* d_out, int out_size, void* d_ws,
                              size_t ws_size, hipStream_t stream) {
  Params p;
  p.x = (const unsigned short*)d_in[0];
  p.kc = (const unsigned short*)d_in[1];
  p.vc = (const unsigned short*)d_in[2];
  p.wq = (const unsigned short*)d_in[3];
  p.wo = (const unsigned short*)d_in[4];
  p.w1 = (const unsigned short*)d_in[6];
  p.b1 = (const unsigned short*)d_in[7];
  p.w2 = (const unsigned short*)d_in[8];
  p.b2 = (const unsigned short*)d_in[9];
  p.out = (unsigned short*)d_out;
  p.ws = (unsigned char*)d_ws;
  fused_decode_kernel<<<dim3(NB * NH), dim3(NT), 0, stream>>>(p);
}
```

```cpp
#include <hip/hip_runtime.h>
#include <hip/hip_bf16.h>
#include <stdint.h>

#define MIMIC 1

namespace {

constexpr int NB = 8, DM = 1024, NH = 32, SK = 4096, DH = 128, FF = 3584, HD = NH * DH;
#define CFG_NSW 8
#define CFG_NL 2
#define CFG_XB 3
#define CFG_EB 2
#define FB_BASE 4
#define FB_MAX 20
#define FB_NUM 8
#define FB_N 8
#define FB_SH 9
#define CFG_WIN 1
#define TWIN 2
#define PSLEEP 4
#define KAUX 2
#define VAUX 2
#define WAUX 2
constexpr int NSW = CFG_NSW, NHW = 8, NT = (NSW + NHW) * 64;
constexpr int NL = CFG_NL;
constexpr int CHK = NL * 4;
constexpr int NCH = SK / CHK / NSW;
constexpr int XBE = CFG_XB + CFG_EB + (CFG_EB ? 1 : 0);
constexpr unsigned SPIN_MAX = 200000u;

constexpr int NG = FF / 128;
constexpr size_t WS_G = 0;
constexpr size_t WS_OG = 4096;
constexpr size_t WS_HG = WS_OG + (size_t)NH * NB * 64 * 8;
constexpr size_t WS_QG = WS_HG + (size_t)NG * NB * 64 * 8;
constexpr size_t WS_UG = WS_QG + (size_t)NH * NB * NB * 128 * 8;
constexpr size_t WS_PG = WS_UG + (size_t)NG * NB * NB * 128 * 8;
constexpr size_t WS_PR = WS_PG + (size_t)NB * DM * NH * 8;
constexpr size_t WS_END = WS_PR + 2 * 256 * 128;

typedef __attribute__((ext_vector_type(2))) __bf16 bf16x2;
typedef __attribute__((ext_vector_type(2))) float f32x2;
typedef __attribute__((ext_vector_type(4))) float f32x4;
typedef __attribute__((ext_vector_type(4))) unsigned u32x4;
typedef __attribute__((ext_vector_type(2))) unsigned u32x2;
typedef __attribute__((ext_vector_type(8))) __bf16 bf16x8;
typedef __attribute__((address_space(1))) unsigned gu32;
typedef __attribute__((address_space(1))) unsigned long long gu64;
#define RLX_AGENT __ATOMIC_RELAXED, __HIP_MEMORY_SCOPE_AGENT

__device__ __forceinline__ float dot2(unsigned a, unsigned b, float acc) {
  return __builtin_amdgcn_fdot2_f32_bf16(__builtin_bit_cast(bf16x2, a), __builtin_bit_cast(bf16x2, b), acc, false);
}
__device__ __forceinline__ unsigned pack_bf16(float lo, float hi) {
  f32x2 v = {lo, hi};
  return __builtin_bit_cast(unsigned, __builtin_convertvector(v, bf16x2));
}
__device__ __forceinline__ float bf_lo(unsigned u) { return __builtin_bit_cast(float, u << 16); }
__device__ __forceinline__ float bf_hi(unsigned u) { return __builtin_bit_cast(float, u & 0xffff0000u); }
__device__ __forceinline__ float bfr(float x) { return bf_lo(pack_bf16(x, 0.f)); }
template <int CTRL> __device__ __forceinline__ float dpp(float x) {
  return __builtin_bit_cast(float, __builtin_amdgcn_mov_dpp(__builtin_bit_cast(int, x), CTRL, 0xf, 0xf, true));
}
constexpr int XOR1 = 0xB1, XOR2 = 0x4E, XOR7 = 0x141, XOR8 = 0x128;
__device__ __forceinline__ float row_sum(float x) {
  x += dpp<XOR1>(x); x += dpp<XOR2>(x); x += dpp<XOR7>(x); x += dpp<XOR8>(x); return x;
}
__device__ __forceinline__ float xrow_sum(float x) {
  auto s = __builtin_amdgcn_permlane16_swap(__float_as_uint(x), __float_as_uint(x), false, false);
  x = __uint_as_float(s[0]) + __uint_as_float(s[1]);
  auto t = __builtin_amdgcn_permlane32_swap(__float_as_uint(x), __float_as_uint(x), false, false);
  return __uint_as_float(t[0]) + __uint_as_float(t[1]);
}
__device__ __forceinline__ float xrow_max(float x) {
  auto s = __builtin_amdgcn_permlane16_swap(__float_as_uint(x), __float_as_uint(x), false, false);
  x = fmaxf(__uint_as_float(s[0]), __uint_as_float(s[1]));
  auto t = __builtin_amdgcn_permlane32_swap(__float_as_uint(x), __float_as_uint(x), false, false);
  return fmaxf(__uint_as_float(t[0]), __uint_as_float(t[1]));
}
__device__ __forceinline__ float wave_sum(float x) { return xrow_sum(row_sum(x)); }
__device__ __forceinline__ unsigned perm_lo(unsigned hi_src, unsigned lo_src) { return __builtin_amdgcn_perm(hi_src, lo_src, 0x05040100u); }
__device__ __forceinline__ unsigned perm_hi(unsigned hi_src, unsigned lo_src) { return __builtin_amdgcn_perm(hi_src, lo_src, 0x07060302u); }
__device__ __forceinline__ u32x4 ld16(const void* p) { return *reinterpret_cast<const u32x4*>(p); }
__device__ __forceinline__ u32x4 ld16nt(const void* p) { return __builtin_nontemporal_load(reinterpret_cast<const u32x4*>(p)); }

template <int AUX>
__device__ __forceinline__ u32x4 ldkv(__amdgpu_buffer_rsrc_t rs, unsigned vo, unsigned so, int i) {
  return __builtin_amdgcn_raw_buffer_load_b128(rs, vo + (i & 3) * 1024, so + (i >> 2) * 4096, AUX);
}

struct LnStat { float sub, den; };
__device__ __forceinline__ LnStat ln_stat(const float (&v)[16]) {
  float s = 0.f;
#pragma unroll
  for (int i = 0; i < 16; ++i) s += v[i];
  const float mean = wave_sum(s) * (1.f / DM);
  float q = 0.f;
#pragma unroll
  for (int i = 0; i < 16; ++i) { const float d = v[i] - mean; q = fmaf(d, d, q); }
  const float var = wave_sum(q) * (1.f / DM);
  LnStat st;
  st.sub = bfr(mean);
  st.den = bfr(sqrtf(bfr(bfr(var) + 1e-5f)));
  return st;
}
__device__ __forceinline__ float ln_apply(float x, LnStat st) {
  return bfr(x - st.sub) / st.den;
}
__device__ __forceinline__ void unpack16(u32x4 a, u32x4 b, float (&v)[16]) {
#pragma unroll
  for (int j = 0; j < 4; ++j) { v[2 * j] = bf_lo(a[j]); v[2 * j + 1] = bf_hi(a[j]); v[8 + 2 * j] = bf_lo(b[j]); v[8 + 2 * j + 1] = bf_hi(b[j]); }
}

struct Params {
  const unsigned short* x; const unsigned short* kc; const unsigned short* vc; const unsigned short* wq; const unsigned short* wo;
  const unsigned short* w1; const unsigned short* b1; const unsigned short* w2; const unsigned short* b2;
  unsigned short* out; unsigned char* ws;
};

constexpr int TROW = 68;
constexpr int TILE = 128 * TROW;
constexpr int L_T = 0;
constexpr int L_Q = L_T + 4 * TILE;
constexpr int L_XN = L_Q + 64;
constexpr int L_Y = L_XN + 512;
constexpr int L_H = L_Y + 512;
constexpr int L_D = L_H + 512;
constexpr int L_REC = L_D + 2048;
constexpr int REC = 132;
constexpr int L_CNT = L_REC + NSW * REC;
constexpr int L_END = L_CNT + 16;
enum { C_STREAM = 0, C_XN, C_Y, C_TAG, C_Q, C_T0, C_T1, C_T2, C_T3, C_H, C_D, C_O, C_LV, C_PROG };
constexpr unsigned OOB = 0xfffffff0u;

template <int N> __device__ __forceinline__ void window() {
  __builtin_amdgcn_sched_barrier(0);
  asm volatile("s_waitcnt vmcnt(%0)" ::"i"(N) : "memory");
  __builtin_amdgcn_sched_barrier(0);
}
template <int ROWB, bool WIN>
__device__ __forceinline__ void tile_load(__amdgpu_buffer_rsrc_t rw, unsigned off, u32x4 (&t)[8]) {
#pragma unroll
  for (int i = 0; i < 8; ++i) {
    if (WIN && (i & 1) == 0) window<TWIN>();
    t[i] = __builtin_amdgcn_raw_buffer_load_b128(rw, off, i * 4 * ROWB, WAUX);
  }
}
__device__ __forceinline__ void tile_store(float* tile, int gw, int r, int c, const u32x4 (&t)[8]) {
#pragma unroll
  for (int i = 0; i < 8; ++i) *reinterpret_cast<u32x4*>(tile + (gw * 32 + i * 4 + r) * TROW + c * 4) = t[i];
}
__device__ __forceinline__ void tile_frags(const float* tile, int gw, int r, int c, unsigned (&raw)[32]) {
#pragma unroll
  for (int i = 0; i < 32; ++i) raw[i] = __float_as_uint(tile[((i >> 3) * 32 + r * 8 + (i & 7)) * TROW + gw * 16 + c]);
}
__device__ __forceinline__ void act_frags(const float* act, int r, int c, u32x4 (&af)[4]) {
#pragma unroll
  for (int kk = 0; kk < 4; ++kk) {
    af[kk] = *reinterpret_cast<const u32x4*>(act + (c & 7) * 64 + kk * 16 + r * 4);
    if (c >= 8) af[kk] = u32x4{0u, 0u, 0u, 0u};
  }
}
__device__ __forceinline__ void pair_tile(const unsigned (&raw)[32], u32x4 (&bf)[8]) {
#pragma unroll
  for (int kk = 0; kk < 4; ++kk)
#pragma unroll
    for (int e = 0; e < 4; ++e) {
      bf[kk * 2][e] = perm_lo(raw[kk * 8 + 2 * e + 1], raw[kk * 8 + 2 * e]);
      bf[kk * 2 + 1][e] = perm_hi(raw[kk * 8 + 2 * e + 1], raw[kk * 8 + 2 * e]);
    }
}
__device__ __forceinline__ void mfma_tile(const u32x4 (&af)[4], const u32x4 (&bf)[8], f32x4& d0, f32x4& d1) {
#pragma unroll
  for (int kk = 0; kk < 4; ++kk) {
    d0 = __builtin_amdgcn_mfma_f32_16x16x32_bf16(__builtin_bit_cast(bf16x8, af[kk]), __builtin_bit_cast(bf16x8, bf[kk * 2]), d0, 0, 0, 0);
    d1 = __builtin_amdgcn_mfma_f32_16x16x32_bf16(__builtin_bit_cast(bf16x8, af[kk]), __builtin_bit_cast(bf16x8, bf[kk * 2 + 1]), d1, 0, 0, 0);
  }
}
template <int SLEEP, bool NARROW>
__device__ __forceinline__ void sweep8(__amdgpu_buffer_rsrc_t rs, unsigned off0, unsigned shi, unsigned slo, unsigned tag,
                                       unsigned pend, unsigned (&v)[16]) {
#pragma unroll
  for (int j = 0; j < 16; ++j) v[j] = 0u;
  unsigned act = pend;
  for (unsigned spins = 0; spins < SPIN_MAX; ++spins) {
    u32x4 g[8];
#pragma unroll
    for (int j = 0; j < 8; ++j) {
      const unsigned off = ((act >> j) & 1u) ? off0 + (j >> 1) * shi + (j & 1) * slo : OOB;
      g[j] = __builtin_amdgcn_raw_buffer_load_b128(rs, off, 0, 16);
    }
    __builtin_amdgcn_sched_barrier(0);
    const unsigned before = pend;
#pragma unroll
    for (int j = 0; j < 8; ++j) {
      const bool ok = ((act >> j) & 1u) && g[j][1] == tag && g[j][3] == tag;
      v[2 * j] = ok ? g[j][0] : v[2 * j];
      v[2 * j + 1] = ok ? g[j][2] : v[2 * j + 1];
      pend = ok ? (pend & ~(1u << j)) : pend;
    }
    if (!__any(pend != 0u)) break;
    if (NARROW) {
      const bool was_narrow_hit = __any(pend != before && act != before);
      act = was_narrow_hit ? pend : (pend & (0u - pend));
      if (!was_narrow_hit) __builtin_amdgcn_s_sleep(SLEEP);
    } else {
      act = pend;
      __builtin_amdgcn_s_sleep(SLEEP);
    }
  }
}
__device__ __forceinline__ void put_granule(gu64* g, unsigned tag, unsigned val) {
  __hip_atomic_store(g, ((unsigned long long)tag << 32) | val, RLX_AGENT);
}
__device__ __forceinline__ void put_rows(__amdgpu_buffer_rsrc_t rs, unsigned off, unsigned rstride, unsigned tag, int r,
                                         const f32x4& d0, const f32x4& d1) {
  if (r < 2) {
#pragma unroll
    for (int i = 0; i < 4; ++i)
      __builtin_amdgcn_raw_buffer_store_b128(u32x4{__float_as_uint(d0[i]), tag, __float_as_uint(d1[i]), tag}, rs,
                                             off + (r * 4 + i) * rstride, 0, 16);
  }
}
__device__ __forceinline__ void lds_arrive(unsigned* cnt, int lane) {
  asm volatile("s_waitcnt lgkmcnt(0)" ::: "memory");
  if (lane == 0) __hip_atomic_fetch_add(cnt, 1u, __ATOMIC_RELAXED, __HIP_MEMORY_SCOPE_WORKGROUP);
}
template <int SLEEP>
__device__ __forceinline__ void lds_wait(unsigned* cnt, unsigned n) {
  for (unsigned spins = 0; spins < 50u * SPIN_MAX; ++spins) {
    if (__hip_atomic_load(cnt, __ATOMIC_RELAXED, __HIP_MEMORY_SCOPE_WORKGROUP) >= n) break;
    __builtin_amdgcn_s_sleep(SLEEP);
  }
  asm volatile("" ::: "memory");
}

__device__ __forceinline__ void hold(int lv) {
  __builtin_amdgcn_sched_barrier(0);
#pragma nounroll
  for (int n = lv; n > 0; --n) __builtin_amdgcn_s_sleep(1);
  __builtin_amdgcn_sched_barrier(0);
}
template <bool PF>
__device__ __forceinline__ void attn_step(u32x4 (&Kb)[NL], u32x4 (&Vb)[NL], __amdgpu_buffer_rsrc_t rsk, __amdgpu_buffer_rsrc_t rsv, unsigned so, unsigned vo,
                                          const u32x4& q, float& m, float& l, float (&acc)[8], int lv = 0) {
  float s[NL];
#pragma unroll
  for (int i = 0; i < NL; ++i) {
    float d = dot2(Kb[i][0], q[0], 0.f);
    d = dot2(Kb[i][1], q[1], d); d = dot2(Kb[i][2], q[2], d); d = dot2(Kb[i][3], q[3], d);
    s[i] = d;
  }
  if (PF) {
#pragma unroll
    for (int i = 0; i < NL; ++i) asm volatile("" : "+v"(s[i]));
    hold(lv);
#pragma unroll
    for (int i = 0; i < NL; ++i) Kb[i] = ldkv<KAUX>(rsk, vo, so, i);
  }
  __builtin_amdgcn_sched_barrier(0);
  constexpr float SCALE = 0.08838834764831845f, LOG2E = 1.4426950408889634f;
#pragma unroll
  for (int i = 0; i < NL; ++i) s[i] = row_sum(s[i]);
#pragma unroll
  for (int i = 0; i < NL; i += 2) {
    unsigned u = pack_bf16(s[i], s[i + 1]);
    u = pack_bf16(bf_lo(u) * SCALE, bf_hi(u) * SCALE);
    s[i] = bf_lo(u) * LOG2E; s[i + 1] = bf_hi(u) * LOG2E;
  }
  float mn = m;
#pragma unroll
  for (int i = 0; i < NL; ++i) mn = fmaxf(mn, s[i]);
  const float alpha = __builtin_amdgcn_exp2f(m - mn);
  m = mn;
  float ps = 0.f;
#pragma unroll
  for (int i = 0; i < NL; ++i) { s[i] = __builtin_amdgcn_exp2f(s[i] - mn); ps += s[i]; }
  l = fmaf(l, alpha, ps);
#pragma unroll
  for (int k = 0; k < 8; ++k) acc[k] *= alpha;
#pragma unroll
  for (int i = 0; i < NL; ++i) {
#pragma unroll
    for (int j = 0; j < 4; ++j) {
      acc[2 * j] = fmaf(s[i], bf_lo(Vb[i][j]), acc[2 * j]);
      acc[2 * j + 1] = fmaf(s[i], bf_hi(Vb[i][j]), acc[2 * j + 1]);
    }
  }
  if (PF) {
#pragma unroll
    for (int k = 0; k < 8; ++k) asm volatile("" : "+v"(acc[k]));
    hold(lv);
#pragma unroll
    for (int i = 0; i < NL; ++i) Vb[i] = ldkv<VAUX>(rsv, vo, so, i);
  }
  __builtin_amdgcn_sched_barrier(0);
}

__device__ __forceinline__ void q_phase(const Params& p, float* lds, __amdgpu_buffer_rsrc_t rs, int h, int b, int hw, int lane) {
  unsigned* cnt = reinterpret_cast<unsigned*>(lds) + L_CNT;
  const int r = lane >> 4, c = lane & 15;
  u32x4 bf[8], af[4];
  unsigned raw[32];
  f32x4 d0 = {0.f, 0.f, 0.f, 0.f}, d1 = {0.f, 0.f, 0.f, 0.f};
  lds_wait<1>(cnt + C_T0, 4);
  tile_frags(lds + L_T, hw, r, c, raw);
  pair_tile(raw, bf);
  lds_wait<1>(cnt + C_XN, NHW);
  const unsigned tag = cnt[C_TAG];
  act_frags(lds + L_XN, r, c, af);
  mfma_tile(af, bf, d0, d1);
  const unsigned pbase = (unsigned)WS_QG + (unsigned)h * (64 * 1024);
  put_rows(rs, pbase + (unsigned)(b * 128 + hw * 32 + c * 2) * 8u, 8 * 1024, tag, r, d0, d1);
  if (hw == 0) {
    unsigned gr[16];
    sweep8<1, false>(rs, pbase + (unsigned)b * (8 * 1024) + (unsigned)lane * 16u, 2048, 1024, tag, 0xffu, gr);
    float t0 = 0.f, t1 = 0.f;
#pragma unroll
    for (int j = 0; j < 8; ++j) { t0 += __uint_as_float(gr[2 * j]); t1 += __uint_as_float(gr[2 * j + 1]); }
    reinterpret_cast<unsigned*>(lds)[L_Q + lane] = pack_bf16(t0, t1);
    lds_arrive(cnt + C_Q, lane);
  }
}

__global__ void __launch_bounds__(NT, NT / 256) fused_decode_kernel(Params p) {
  __shared__ __attribute__((aligned(16))) float lds[L_END];
  unsigned* ldsu = reinterpret_cast<unsigned*>(lds);
  const int bid = blockIdx.x, xcd = bid & 7, idx = bid >> 3;
  const int h = xcd * 4 + (idx >> 3), b = idx & 7;
  const int tid = threadIdx.x, lane = tid & 63, wave = __builtin_amdgcn_readfirstlane(tid >> 6);
  const int r = lane >> 4, c = lane & 15;
  const bool streamer = wave >= NHW;

  if (streamer) {
    const int sw = wave - NHW;
    const size_t slab = (size_t)(b * NH + h) * SK * DH;
    const __amdgpu_buffer_rsrc_t rsk = __builtin_amdgcn_make_buffer_rsrc((void*)(p.kc + slab), 0, SK * DH * 2, 0x00020000);
    const __amdgpu_buffer_rsrc_t rsv = __builtin_amdgcn_make_buffer_rsrc((void*)(p.vc + slab), 0, SK * DH * 2, 0x00020000);
    unsigned so = (unsigned)sw * (CHK * DH * 2);
    const unsigned vo = (unsigned)lane * 16u;
    constexpr int STRIDE = NSW * CHK * DH * 2;
    constexpr int XB = CFG_XB;
    u32x4 Kb[NL], Vb[NL], Kx[XB > 0 ? XB : 1][NL], Vx[XB > 0 ? XB : 1][NL];
    __syncthreads();
#pragma unroll
    for (int i = 0; i < NL; ++i) Kb[i] = ldkv<KAUX>(rsk, vo, so, i);
#pragma unroll
    for (int i = 0; i < NL; ++i) Vb[i] = ldkv<VAUX>(rsv, vo, so, i);
#pragma unroll
    for (int x = 0; x < XB; ++x) {
      window<NL>();
#pragma unroll
      for (int i = 0; i < NL; ++i) Kx[x][i] = ldkv<KAUX>(rsk, vo, so + (NCH - 1 - x) * STRIDE, i);
      window<NL>();
#pragma unroll
      for (int i = 0; i < NL; ++i) Vx[x][i] = ldkv<VAUX>(rsv, vo, so + (NCH - 1 - x) * STRIDE, i);
    }
    __builtin_amdgcn_sched_barrier(0);
    lds_wait<2>(ldsu + L_CNT + C_Q, 1);
    const unsigned tag = ldsu[L_CNT + C_TAG];
    const u32x4 q = *reinterpret_cast<const u32x4*>(lds + L_Q + c * 4);
    float m = -INFINITY, l = 0.f, acc[8];
#pragma unroll
    for (int k = 0; k < 8; ++k) acc[k] = 0.f;
    if (XB > 0) {
      so += STRIDE;
      attn_step<true>(Kb, Vb, rsk, rsv, so, vo, q, m, l, acc);
#pragma unroll
      for (int x = 0; x < XB; ++x) attn_step<false>(Kx[x], Vx[x], rsk, rsv, so, vo, q, m, l, acc);
    }
    constexpr int EB = CFG_EB;
    const unsigned eo = (unsigned)sw * (CHK * DH * 2) + (NCH - 1 - XB) * STRIDE;
    for (int it = 1; it < NCH - 1 - XBE; ++it) {
      so += STRIDE;
      const int lv = __builtin_amdgcn_readfirstlane(__hip_atomic_load(ldsu + L_CNT + C_LV, __ATOMIC_RELAXED, __HIP_MEMORY_SCOPE_WORKGROUP));
      if (lane == 0) __hip_atomic_fetch_add(ldsu + L_CNT + C_PROG, 1u, __ATOMIC_RELAXED, __HIP_MEMORY_SCOPE_WORKGROUP);
      attn_step<true>(Kb, Vb, rsk, rsv, so, vo, q, m, l, acc, lv);
    }
    if (EB) {
#pragma unroll
      for (int x = 0; x < EB; ++x) {
#pragma unroll
        for (int i = 0; i < NL; ++i) Kx[x][i] = ldkv<KAUX>(rsk, vo, eo - x * STRIDE, i);
#pragma unroll
        for (int i = 0; i < NL; ++i) Vx[x][i] = ldkv<VAUX>(rsv, vo, eo - x * STRIDE, i);
        __builtin_amdgcn_sched_barrier(0);
        if (x == EB - 1) {
          so += STRIDE;
          attn_step<true>(Kb, Vb, rsk, rsv, so, vo, q, m, l, acc);
        }
      }
#pragma unroll
      for (int x = 0; x < EB; ++x) attn_step<false>(Kx[x], Vx[x], rsk, rsv, so, vo, q, m, l, acc);
    }
    attn_step<false>(Kb, Vb, rsk, rsv, so, vo, q, m, l, acc);
    const float M = xrow_max(m);
    const float wt = __builtin_amdgcn_exp2f(m - M);
    l = xrow_sum(l * wt);
#pragma unroll
    for (int k = 0; k < 8; ++k) acc[k] = xrow_sum(acc[k] * wt);
    float* rec = lds + L_REC + sw * REC;
    if (r == 0) {
      f32x4* d = reinterpret_cast<f32x4*>(rec + 4 + c * 8);
      d[0] = f32x4{acc[0], acc[1], acc[2], acc[3]}; d[1] = f32x4{acc[4], acc[5], acc[6], acc[7]};
      if (c == 0) { rec[0] = M; rec[1] = l; }
    }
    unsigned arrived = 0;
    if (lane == 0) arrived = __hip_atomic_fetch_add(ldsu + L_CNT + C_STREAM, 1u, __ATOMIC_ACQ_REL, __HIP_MEMORY_SCOPE_WORKGROUP);
    arrived = __builtin_amdgcn_readfirstlane(arrived);
    if (arrived == NSW - 1) {
      __builtin_amdgcn_fence(__ATOMIC_ACQUIRE, "workgroup");
      float MM = -INFINITY;
#pragma unroll
      for (int w = 0; w < NSW; ++w) MM = fmaxf(MM, lds[L_REC + w * REC]);
      float L = 0.f, o0 = 0.f, o1 = 0.f;
#pragma unroll
      for (int w = 0; w < NSW; ++w) {
        const float* rw = lds + L_REC + w * REC;
        const float e = __builtin_amdgcn_exp2f(rw[0] - MM);
        const f32x2 a = *reinterpret_cast<const f32x2*>(rw + 4 + 2 * lane);
        L = fmaf(e, rw[1], L); o0 = fmaf(e, a[0], o0); o1 = fmaf(e, a[1], o1);
      }
      const float inv = 1.f / L;
      const unsigned pay = pack_bf16(o0 * inv, o1 * inv);
      gu64* og = (gu64*)(p.ws + WS_OG) + (size_t)(h * NB + b) * 64 + lane;
      __hip_atomic_store(og, ((unsigned long long)tag << 32) | pay, RLX_AGENT);
    }
    return;
  }

  const int hw = wave, gw = wave & 3;
  const bool grpA = hw < 4;
  __builtin_amdgcn_s_setprio(3);
  unsigned* cnt = ldsu + L_CNT;
  const __amdgpu_buffer_rsrc_t rs = __builtin_amdgcn_make_buffer_rsrc(p.ws, 0, (int)WS_END, 0x00020000);
  const bool mlp = idx < 24 || (xcd & 1) == 0;
  const int g = idx < 24 ? xcd * 3 + (idx >> 3) : 24 + (xcd >> 1);

  const unsigned short* xr = p.x + hw * DM + lane * 16;
  const u32x4 x0 = ld16(xr), x1 = ld16(xr + 8);
  const unsigned g0 = __hip_atomic_load((gu32*)(p.ws + WS_G), RLX_AGENT);
  u32x4 ta[8], tb[8];
  if (grpA) {
    const __amdgpu_buffer_rsrc_t r0 = __builtin_amdgcn_make_buffer_rsrc((void*)p.wq, 0, DM * HD * 2, 0x00020000);
    tile_load<HD * 2, false>(r0, (unsigned)((b * 128 + gw * 32 + r) * HD + h * 128 + c * 8) * 2u, ta);
  }
  const int lv0 = (xcd & 1) ? 0 : FB_BASE;
  if (tid < 16) cnt[tid] = tid == C_LV ? lv0 : 0u;
  __builtin_amdgcn_sched_barrier(0);
  __syncthreads();
  {
    float v[16];
    unpack16(x0, x1, v);
    const LnStat s1 = ln_stat(v);
    u32x4 y0, y1;
#pragma unroll
    for (int j = 0; j < 4; ++j) {
      y0[j] = pack_bf16(ln_apply(v[2 * j], s1), ln_apply(v[2 * j + 1], s1));
      y1[j] = pack_bf16(ln_apply(v[8 + 2 * j], s1), ln_apply(v[8 + 2 * j + 1], s1));
    }
    if ((lane >> 3) == b) {
      u32x4* xd = reinterpret_cast<u32x4*>(lds + L_XN + hw * 64 + (lane & 7) * 8);
      xd[0] = y0; xd[1] = y1;
    }
    if (tid == 0) cnt[C_TAG] = g0 + 1u;
    lds_arrive(cnt + C_XN, lane);
    if (grpA) {
      tile_store(lds + L_T, gw, r, c, ta);
      lds_arrive(cnt + C_T0, lane);
      q_phase(p, lds, rs, h, b, hw, lane);
      const __amdgpu_buffer_rsrc_t r1 = __builtin_amdgcn_make_buffer_rsrc((void*)p.w2, 0, mlp ? FF * DM * 2 : 0, 0x00020000);
      tile_load<DM * 2, true>(r1, (unsigned)((g * 128 + gw * 32 + r) * DM + b * 128 + c * 8) * 2u, tb);
    }
    __builtin_amdgcn_sched_barrier(0);
    unpack16(y0, y1, v);
    const LnStat s2 = ln_stat(v);
#pragma unroll
    for (int j = 0; j < 4; ++j) {
      y0[j] = pack_bf16(ln_apply(v[2 * j], s2), ln_apply(v[2 * j + 1], s2));
      y1[j] = pack_bf16(ln_apply(v[8 + 2 * j], s2), ln_apply(v[8 + 2 * j + 1], s2));
    }
    if ((lane >> 3) == b) {
      u32x4* yd = reinterpret_cast<u32x4*>(lds + L_Y + hw * 64 + (lane & 7) * 8);
      yd[0] = y0; yd[1] = y1;
    }
    lds_arrive(cnt + C_Y, lane);
    if (!grpA) {
      const __amdgpu_buffer_rsrc_t r0 = __builtin_amdgcn_make_buffer_rsrc((void*)p.w1, 0, mlp ? DM * FF * 2 : 0, 0x00020000);
      tile_load<FF * 2, true>(r0, (unsigned)((b * 128 + gw * 32 + r) * FF + g * 128 + c * 8) * 2u, ta);
      const __amdgpu_buffer_rsrc_t r1 = __builtin_amdgcn_make_buffer_rsrc((void*)p.wo, 0, HD * DM * 2, 0x00020000);
      tile_load<DM * 2, true>(r1, (unsigned)((h * 128 + gw * 32 + r) * DM + b * 128 + c * 8) * 2u, tb);
      tile_store(lds + L_T + TILE, gw, r, c, ta);
      lds_arrive(cnt + C_T1, lane);
    }
  }
  u32x4 bf[8], af[4];
  unsigned raw[32], gr[16];
  f32x4 d0 = {0.f, 0.f, 0.f, 0.f}, d1 = {0.f, 0.f, 0.f, 0.f};
  lds_wait<1>(cnt + C_XN, NHW);
  const unsigned tag = cnt[C_TAG];
  const unsigned pbase = (unsigned)WS_UG + (unsigned)g * (64 * 1024);
  if (!grpA) {
    lds_wait<2>(cnt + C_T1, 4);
    tile_frags(lds + L_T + TILE, gw, r, c, raw);
    pair_tile(raw, bf);
    lds_wait<2>(cnt + C_Y, NHW);
    act_frags(lds + L_Y, r, c, af);
    mfma_tile(af, bf, d0, d1);
    if (mlp) put_rows(rs, pbase + (unsigned)(b * 128 + gw * 32 + c * 2) * 8u, 8 * 1024, tag, r, d0, d1);
  }
  tile_store(lds + L_T + (grpA ? 2 : 3) * TILE, gw, r, c, tb);
  lds_arrive(cnt + (grpA ? C_T2 : C_T3), lane);
  if (hw == 4 && mlp) {
    const unsigned b1p = *reinterpret_cast<const unsigned*>(p.b1 + g * 128 + lane * 2);
    __builtin_amdgcn_s_sleep(64);
    sweep8<64, true>(rs, pbase + (unsigned)b * (8 * 1024) + (unsigned)lane * 16u, 2048, 1024, tag, 0xffu, gr);
    float t0 = 0.f, t1 = 0.f;
#pragma unroll
    for (int j = 0; j < 8; ++j) { t0 += __uint_as_float(gr[2 * j]); t1 += __uint_as_float(gr[2 * j + 1]); }
    t0 = bfr(bfr(t0) + bf_lo(b1p)); t1 = bfr(bfr(t1) + bf_hi(b1p));
    const float e0 = 0.5f * t0 * (1.f + tanhf(0.7978845608028654f * (t0 + 0.044715f * t0 * t0 * t0)));
    const float e1 = 0.5f * t1 * (1.f + tanhf(0.7978845608028654f * (t1 + 0.044715f * t1 * t1 * t1)));
    put_granule((gu64*)(p.ws + WS_HG) + (size_t)(g * NB + b) * 64 + lane, tag, pack_bf16(e0, e1));
  }
  lds_wait<2>(cnt + (grpA ? C_T2 : C_T3), 4);
  tile_frags(lds + L_T + (grpA ? 2 : 3) * TILE, gw, r, c, raw);
  pair_tile(raw, bf);
#pragma unroll
  for (int k = 0; k < 8; ++k) asm volatile("" : "+v"(bf[k]));
  d0 = f32x4{0.f, 0.f, 0.f, 0.f}; d1 = f32x4{0.f, 0.f, 0.f, 0.f};
  f32x4* dx = reinterpret_cast<f32x4*>(lds + L_D + (gw * 64 + lane) * 8);
  if (grpA) {
    if (mlp) {
      if (hw == 0) {
        __builtin_amdgcn_s_sleep(127); __builtin_amdgcn_s_sleep(127);
        sweep8<127, true>(rs, (unsigned)WS_HG + (unsigned)g * 4096u + (unsigned)lane * 16u, 2048, 1024, tag, 0x0fu, gr);
#pragma unroll
        for (int j = 0; j < 4; ++j) *reinterpret_cast<u32x2*>(lds + L_H + j * 128 + lane * 2) = u32x2{gr[2 * j], gr[2 * j + 1]};
        lds_arrive(cnt + C_H, lane);
      }
      lds_wait<32>(cnt + C_H, 1);
      act_frags(lds + L_H, r, c, af);
      mfma_tile(af, bf, d0, d1);
    }
    dx[0] = d0; dx[1] = d1;
    lds_arrive(cnt + C_D, lane);
    if (hw == 1) {
      unsigned prev = 0u, ptk = 0u;
      for (int k = 0; k < 256; ++k) {
        unsigned tk = (unsigned)__builtin_amdgcn_s_memrealtime() >> FB_SH;
        const unsigned tk0 = tk;
        constexpr unsigned DONE = NSW * (NCH - 3 - XBE);
        unsigned own = 0u;
        for (int n = 0; n < 4096 && tk == tk0 && own < DONE; ++n) {
          __builtin_amdgcn_s_sleep(2);
          tk = (unsigned)__builtin_amdgcn_s_memrealtime() >> FB_SH;
          own = __builtin_amdgcn_readfirstlane(__hip_atomic_load(cnt + C_PROG, __ATOMIC_RELAXED, __HIP_MEMORY_SCOPE_WORKGROUP));
        }
        if (own >= DONE) break;
        __builtin_amdgcn_raw_buffer_store_b32((((tag << 4) | (tk & 15u)) << 10) | own, rs,
                                              lane < 32 ? (unsigned)WS_PR + ((tk & 1u) * 256u + bid) * 128u + lane * 4u : OOB, 0, 16);
        const unsigned e = __builtin_amdgcn_raw_buffer_load_b32(
            rs, lane < FB_N ? (unsigned)WS_PR + ((~tk & 1u) * 256u + ((idx + tk * 5u + xcd * 4u + (lane >> 3) * 16u) & 31u) * 8u + (lane & 7)) * 128u : OOB, 0, 16);
        const bool ok = lane < FB_N && (e >> 10) == (((tag << 4) | ((tk - 1u) & 15u)) & 0x3fffffu);
        const int nv = __popcll(__ballot(ok));
        const float sum = wave_sum(ok ? (float)(e & 1023u) : 0.f);
        int lv = -1;
        if (nv > FB_N / 2 && k > 0 && ptk + 1u == tk) {
          const float d = (float)prev - sum / (float)nv;
          lv = min(max(lv0 + (int)(d * (FB_NUM * 0.25f)), 0), FB_MAX);
        } else if (own > (unsigned)(NSW * NCH / 2)) {
          lv = 0;
        }
        if (lane == 0 && lv >= 0) __hip_atomic_store(cnt + C_LV, (unsigned)lv, __ATOMIC_RELAXED, __HIP_MEMORY_SCOPE_WORKGROUP);
        prev = own; ptk = tk;
      }
    }
    if (hw == 0) {
      const int ob = bid & 7, on = (bid >> 3) * 32 + c * 2;
      const unsigned xv = *reinterpret_cast<const unsigned*>(p.x + ob * DM + on);
      const unsigned bv = *reinterpret_cast<const unsigned*>(p.b2 + on);
      lds_wait<4>(cnt + C_O, 1);
      sweep8<PSLEEP, true>(rs, (unsigned)(WS_PG + ((size_t)((ob * 32 + (bid >> 3)) * NH + r * 8) * 32 + c * 2) * 8), 512, 256, tag, 0xffu, gr);
      float t0 = 0.f, t1 = 0.f;
#pragma unroll
      for (int j = 0; j < 8; ++j) { t0 += __uint_as_float(gr[2 * j]); t1 += __uint_as_float(gr[2 * j + 1]); }
      t0 = xrow_sum(t0) + (bf_lo(xv) + bf_lo(bv));
      t1 = xrow_sum(t1) + (bf_hi(xv) + bf_hi(bv));
      if (r == 0) *reinterpret_cast<unsigned*>(p.out + ob * DM + on) = pack_bf16(t0, t1);
      if (bid == 0 && lane == 0) __hip_atomic_store((gu32*)(p.ws + WS_G), tag, RLX_AGENT);
    }
  } else {
    lds_wait<32>(cnt + C_D, 4);
    d0 = dx[0]; d1 = dx[1];
    if (hw == 4) {
      lds_wait<4>(cnt + C_STREAM, NSW);
      sweep8<PSLEEP, true>(rs, (unsigned)WS_OG + (unsigned)h * 4096u + (unsigned)lane * 16u, 2048, 1024, tag, 0x0fu, gr);
#pragma unroll
      for (int j = 0; j < 4; ++j) *reinterpret_cast<u32x2*>(lds + L_H + j * 128 + lane * 2) = u32x2{gr[2 * j], gr[2 * j + 1]};
      lds_arrive(cnt + C_O, lane);
    } else {
      lds_wait<8>(cnt + C_STREAM, NSW);
    }
    lds_wait<1>(cnt + C_O, 1);
    act_frags(lds + L_H, r, c, af);
    mfma_tile(af, bf, d0, d1);
    put_rows(rs, (unsigned)WS_PG + (unsigned)(((b * 4 + gw) * NH + h) * 32 + c * 2) * 8u, 32 * NH * 32 * 8, tag, r, d0, d1);
  }
}

}

extern "C" void kernel_launch(void* const* d_in, const int* in_sizes, int n_in, void* d_out, int out_size, void* d_ws,
                              size_t ws_size, hipStream_t stream) {
  Params p;
  p.x = (const unsigned short*)d_in[0];
  p.kc = (const unsigned short*)d_in[1];
  p.vc = (const unsigned short*)d_in[2];
  p.wq = (const unsigned short*)d_in[3];
  p.wo = (const unsigned short*)d_in[4];
  p.w1 = (const unsigned short*)d_in[6];
  p.b1 = (const unsigned short*)d_in[7];
  p.w2 = (const unsigned short*)d_in[8];
  p.b2 = (const unsigned short*)d_in[9];
  p.out = (unsigned short*)d_out;
  p.ws = (unsigned char*)d_ws;
  fused_decode_kernel<<<dim3(NB * NH), dim3(NT), 0, stream>>>(p);
}
```
